```python
import torch

def _softmax(x, axis=-1):
    return torch.softmax(x, dim=axis)
def _gelu(x):
    return torch.nn.functional.gelu(x, approximate="tanh")
def _silu(x):
    return torch.nn.functional.silu(x)
def _sigmoid(x):
    return torch.sigmoid(x)
def _layernorm(x, axis=-1, eps=1e-5):
    m = x.mean(dim=axis, keepdim=True)
    v = x.var(dim=axis, keepdim=True, unbiased=False)
    return (x - m) / torch.sqrt(v + eps)
def _scatter_add(tgt, idx, src, axis=0):
    return tgt.clone().index_add_(axis, idx, src)
def _segment_sum(src, seg, n):
    out = torch.zeros((n,) + src.shape[1:], dtype=src.dtype)
    return out.index_add_(0, seg, src)
def _pad(x, pw):
    flat = tuple(v for pair in reversed(pw) for v in pair)
    return torch.nn.functional.pad(x, flat)

kernel_name = 'parallel_sum__ssm_conv_h3__mixer_channel_mlp__prompt'

def setup_inputs():

    _w = (lambda a: (lambda t: t.to(torch.bfloat16) if t.is_floating_point() else t)(torch.as_tensor(a)))
    def _A_setup():
        B, S, D = 1, 2048, 512
        ks = 4
        return {
            "q": _w((lambda *s: torch.randn(*s, dtype=torch.bfloat16) * 0.05)(B, S, D)),
            "k": _w((lambda *s: torch.randn(*s, dtype=torch.bfloat16) * 0.2)(B, S, D)),
            "v": _w((lambda *s: torch.randn(*s, dtype=torch.bfloat16) * 0.2)(B, S, D)),
            "shift_k": _w((lambda *s: torch.randn(*s, dtype=torch.bfloat16) * 0.2)(D, ks) * 0.5),
            "ssm_k": _w((lambda *s: torch.randn(*s, dtype=torch.bfloat16) * 0.8)(D, S) * (1.0 / S ** 0.5)),
            "ssm_D": _w((lambda *s: torch.randn(*s, dtype=torch.bfloat16) * 0.2)(D)),
        }
    _A_in = _A_setup()

    def _B_setup():
        B, S, D, F = 1, 2048, 512, 1792
        return {
            "x": _w((lambda *s: torch.randn(*s, dtype=torch.bfloat16) * 0.05)(B, S, D)),
            "w1": _w((lambda *s: torch.randn(*s, dtype=torch.bfloat16) * 0.8)(D, F) * (D ** -0.5)),
            "b1": _w((lambda *s: torch.randn(*s, dtype=torch.bfloat16) * 0.4)(F)),
            "w2": _w((lambda *s: torch.randn(*s, dtype=torch.bfloat16) * 0.05)(F, D) * (F ** -0.5)),
            "b2": _w((lambda *s: torch.randn(*s, dtype=torch.bfloat16) * 0.05)(D)),
        }
    _B_in = _B_setup()

    return {
        "A_q": _A_in["q"],
        "A_k": _A_in["k"],
        "A_v": _A_in["v"],
        "A_shift_k": _A_in["shift_k"],
        "A_ssm_k": _A_in["ssm_k"],
        "A_ssm_D": _A_in["ssm_D"],
        "B_x": _B_in["x"],
        "B_w1": _B_in["w1"],
        "B_b1": _B_in["b1"],
        "B_w2": _B_in["w2"],
        "B_b2": _B_in["b2"],
    }

def reference(A_q, A_k, A_v, A_shift_k, A_ssm_k, A_ssm_D, B_x, B_w1, B_b1, B_w2, B_b2):
    def _A_ref(q, k, v, shift_k, ssm_k, ssm_D):
        B, S, D = q.shape
        ks = shift_k.shape[-1]
        k_pad = _pad(k, ((0, 0), (ks - 1, 0), (0, 0)))
        k_shift = torch.zeros_like(k)
        for t in range(ks):
            k_shift = k_shift + k_pad[:, t:t + S, :] * shift_k[:, ks - 1 - t][None, None, :]
        kv = k_shift * v
        L = 2 * S
        kv_pad = _pad(kv, ((0, 0), (0, S), (0, 0)))
        kf_pad = _pad(ssm_k, ((0, 0), (0, S)))
        kv_f = (lambda x, axes=(-2,-1): torch.fft.rfft2(x.float(), dim=axes))(kv_pad[:, None, :, :], axes=(1, 2))
        kf_f = (lambda x, axes=(-2,-1): torch.fft.rfft2(x.float(), dim=axes))(kf_pad[:, None, :], axes=(1, 2))
        kf_f = (lambda x, *d: x.permute(*d))(kf_f, 1, 2, 0)
        y_f = kv_f * kf_f[None, :, :, :]
        y = (lambda x, s, axes=(-2,-1): torch.fft.irfft2(x, s=s, dim=axes).to(torch.bfloat16))(y_f, (1, L), axes=(1, 2))[:, 0, :S, :]
        y = y + kv * ssm_D[None, None, :]
        return q * y

    def _B_ref(x, w1, b1, w2, b2):
        y = _layernorm(x, axis=-1)
        h = _gelu(torch.einsum("bsd,df->bsf", y, w1) + b1)
        return torch.einsum("bsf,fd->bsd", h, w2) + b2

    x = A_q
    xn = _layernorm(x, axis=-1)
    return x + _A_ref(xn, A_k, A_v, A_shift_k, A_ssm_k, A_ssm_D) + _B_ref(xn, B_w1, B_b1, B_w2, B_b2)
```

```cpp
#include <hip/hip_runtime.h>
#include <hip/hip_bf16.h>
typedef unsigned short u16;
constexpr int S=2048, D=512, F=1792;
__device__ __forceinline__ float b2f(u16 x){ return __uint_as_float(((unsigned)x)<<16); }
__device__ __forceinline__ u16 f2b(float f){ __hip_bfloat16 h = __float2bfloat16(f); return *(u16*)&h; }
__device__ __forceinline__ float rb(float f){ return b2f(f2b(f)); }

__global__ void k_prep(const u16* q,const u16* k,const u16* v,const u16* sk,u16* xn,u16* y2,u16* kv){
  int s=blockIdx.x, t=threadIdx.x;
  __shared__ float red[16];
  float x=b2f(q[s*D+t]);
  auto bsum=[&](float a){ for(int o=32;o;o>>=1)a+=__shfl_xor(a,o); __syncthreads(); if((t&63)==0)red[t>>6]=a; __syncthreads(); float r=0; for(int i=0;i<8;i++)r+=red[i]; return r;};
  float m=bsum(x)/D; float dv=x-m; float var=bsum(dv*dv)/D;
  float a=rb(rb(x-rb(m))/rb(sqrtf(rb(rb(var)+1e-5f))));
  xn[s*D+t]=f2b(a);
  float m2=bsum(a)/D; float d2=a-m2; float var2=bsum(d2*d2)/D;
  float b=rb(rb(a-rb(m2))/rb(sqrtf(rb(rb(var2)+1e-5f))));
  y2[s*D+t]=f2b(b);
  float ks=0;
  for(int j=3;j>=0;j--){ float kk=(s-j>=0)?b2f(k[(s-j)*D+t]):0.f; ks=rb(ks+rb(kk*b2f(sk[t*4+j]))); }
  kv[s*D+t]=f2b(ks*b2f(v[s*D+t]));
}
__global__ void k_conv(const u16* kv,const u16* h,const u16* sD,u16* y){
  int s=blockIdx.x, d=threadIdx.x;
  float acc=0;
  for(int j=0;j<=s;j++) acc+=b2f(kv[(s-j)*D+d])*b2f(h[d*S+j]);
  float r=rb(rb(acc)+rb(b2f(kv[s*D+d])*b2f(sD[d])));
  y[s*D+d]=f2b(r);
}
__global__ void k_g1(const u16* y2,const u16* w1,const u16* b1,u16* h){
  int s=blockIdx.x;
  for(int f=threadIdx.x;f<F;f+=blockDim.x){
    float acc=0; for(int k=0;k<D;k++) acc+=b2f(y2[s*D+k])*b2f(w1[k*F+f]);
    float z=rb(rb(acc)+b2f(b1[f]));
    float g=0.5f*z*(1.f+tanhf(0.7978845608028654f*(z+0.044715f*z*z*z)));
    h[s*F+f]=f2b(g);
  }
}
__global__ void k_g2(const u16* h,const u16* w2,const u16* b2,const u16* q,const u16* xn,const u16* y,u16* out){
  int s=blockIdx.x, d=threadIdx.x;
  float acc=0; for(int k=0;k<F;k++) acc+=b2f(h[s*F+k])*b2f(w2[k*D+d]);
  float B=rb(rb(acc)+b2f(b2[d]));
  float A=rb(b2f(xn[s*D+d])*b2f(y[s*D+d]));
  out[s*D+d]=f2b(rb(b2f(q[s*D+d])+A)+B);
}
extern "C" void kernel_launch(void* const* d_in, const int* in_sizes, int n_in, void* d_out, int out_size, void* d_ws, size_t ws_size, hipStream_t stream){
  const u16* q=(const u16*)d_in[0],*k=(const u16*)d_in[1],*v=(const u16*)d_in[2],*sk=(const u16*)d_in[3],*hk=(const u16*)d_in[4],*sD=(const u16*)d_in[5];
  const u16* w1=(const u16*)d_in[7],*b1=(const u16*)d_in[8],*w2=(const u16*)d_in[9],*b2=(const u16*)d_in[10];
  u16* ws=(u16*)d_ws; u16* xn=ws,*y2=ws+S*D,*kv=ws+2*S*D,*y=ws+3*S*D,*h=ws+4*S*D;
  k_prep<<<S,512,0,stream>>>(q,k,v,sk,xn,y2,kv);
  k_conv<<<S,512,0,stream>>>(kv,hk,sD,y);
  k_g1<<<S,256,0,stream>>>(y2,w1,b1,h);
  k_g2<<<S,512,0,stream>>>(h,w2,b2,q,xn,y,(u16*)d_out);
}
```

```cpp
#include <hip/hip_runtime.h>
#include <hip/hip_bf16.h>
typedef unsigned short u16;
typedef unsigned v4u __attribute__((ext_vector_type(4)));
typedef unsigned v2u __attribute__((ext_vector_type(2)));
typedef float f32x16 __attribute__((ext_vector_type(16)));
typedef float f32x4 __attribute__((ext_vector_type(4)));
typedef float f32x2 __attribute__((ext_vector_type(2)));
typedef short s16x4 __attribute__((ext_vector_type(4)));
typedef __bf16 bf16x8 __attribute__((ext_vector_type(8)));
typedef __bf16 bf16x2 __attribute__((ext_vector_type(2)));
#define GAS __attribute__((address_space(1)))
#define LAS __attribute__((address_space(3)))
#define DI __device__ __forceinline__
constexpr int S=2048, D=512, F=1792;
#define AUX1 0
#define AUXW 0
constexpr size_t WS_UT=0, WS_YT=2u<<20, WS_H=4u<<20, WS_ST=12u<<20, WS_CS=WS_ST+65536;

DI unsigned cvtpk(float lo,float hi){ f32x2 v={lo,hi}; return __builtin_bit_cast(unsigned,__builtin_convertvector(v,bf16x2)); }
DI float blo(unsigned x){ return __uint_as_float(x<<16); }
DI float bhi(unsigned x){ return __uint_as_float(x&0xffff0000u); }
DI float dot2(unsigned a,unsigned b,float c){ return __builtin_amdgcn_fdot2_f32_bf16(__builtin_bit_cast(bf16x2,a),__builtin_bit_cast(bf16x2,b),c,false); }
DI v2u trd(const char* p){ return __builtin_bit_cast(v2u,__builtin_amdgcn_ds_read_tr16_b64_v4i16((LAS s16x4*)p)); }
DI f32x16 mfma32(v4u a,v4u b,f32x16 c){ return __builtin_amdgcn_mfma_f32_32x32x16_bf16(__builtin_bit_cast(bf16x8,a),__builtin_bit_cast(bf16x8,b),c,0,0,0); }
DI f32x4 mfma16(v4u a,v4u b,f32x4 c){ return __builtin_amdgcn_mfma_f32_16x16x32_bf16(__builtin_bit_cast(bf16x8,a),__builtin_bit_cast(bf16x8,b),c,0,0,0); }
template<int AUX> DI void glds(const void* g, char* l){ __builtin_amdgcn_global_load_lds((const GAS void*)g,(LAS void*)l,16,0,AUX); }
DI float wsum(float a){ for(int o=32;o;o>>=1) a+=__shfl_xor(a,o); return a; }

__global__ __launch_bounds__(256) void kA(const char* q,const char* k,const char* v,const u16* sk,const char* w1,char* ws){
  __shared__ __attribute__((aligned(16))) char sm[9216+8192+8192+1024*8];
  const int tid=threadIdx.x, l=tid&63, w=__builtin_amdgcn_readfirstlane(tid>>6), b=blockIdx.x;
  if(b>=256){
    int cb=b-256; float a[8]={};
    for(int i=0;i<16;i++){ int r=i*32+(tid>>3); v4u x=*(const v4u*)(w1+(size_t)r*3584+cb*128+(tid&7)*16);
      for(int j=0;j<4;j++){ a[2*j]+=blo(x[j]); a[2*j+1]+=bhi(x[j]); } }
    float* red=(float*)sm;
    for(int j=0;j<8;j++) red[(tid>>3)*64+(tid&7)*8+j]=a[j];
    __syncthreads();
    if(tid<64){ float s=0; for(int i=0;i<32;i++) s+=red[i*64+tid]; ((float*)(ws+WS_CS))[cb*64+tid]=s; }
    return;
  }
  const int sb=b>>3, db=b&7;
  char* K=sm; char* V=sm+9216; char* O=sm+9216+8192;
  const __amdgpu_buffer_rsrc_t rk=__builtin_amdgcn_make_buffer_rsrc((void*)k,0,S*D*2,0x00020000);
  const int srcch=(l&7)^(((l>>4)&1)<<2);
  for(int p=w;p<9;p+=4){ int r=sb*64-8+p*8+(l>>3);
    __builtin_amdgcn_raw_ptr_buffer_load_lds(rk,(LAS void*)(K+p*1024),16,r*1024+db*128+srcch*16,0,0,AUX1); }
  for(int p=w;p<8;p+=4){ int r=sb*64+p*8+(l>>3); glds<AUX1>(v+(size_t)r*1024+db*128+srcch*16,V+p*1024); }
  v4u xr[2]; for(int j=0;j<2;j++) xr[j]=*(const v4u*)(q+(size_t)(b*8+w*2+j)*1024+l*16);
  v2u skv=*(const v2u*)(sk+(db*64+l)*4);
  for(int j=0;j<2;j++){ float s1=0,s2=0; for(int e=0;e<4;e++){ s1=dot2(xr[j][e],0x3f803f80u,s1); s2=dot2(xr[j][e],xr[j][e],s2);}
    s1=wsum(s1); s2=wsum(s2); float m=s1*(1.f/D); float var=fmaxf(s2*(1.f/D)-m*m,0.f);
    float r1=rsqrtf(var+1e-5f); float var2=var*r1*r1; float r=r1*rsqrtf(var2+1e-5f);
    if(l==0) *(f32x4*)(ws+WS_ST+(b*8+w*2+j)*16)=f32x4{m,r1,r,0.f}; }
  __syncthreads();
  const int G=l>>4,qq=(l&15)>>2,p=l&3;
  const int la=qq*128+((((G>>1)^(qq>>1))&1)*64)+(G&1)*32+p*8;
  unsigned kp[10],vp[8];
  for(int i=0;i<5;i++){ v2u t=trd(K+(4+16*w+4*i)*128+la); kp[2*i]=t.x; kp[2*i+1]=t.y; }
  for(int i=0;i<4;i++){ v2u t=trd(V+(16*w+4*i)*128+la); vp[2*i]=t.x; vp[2*i+1]=t.y; }
  const unsigned s0=skv.x&0xffff,s1=skv.x>>16,s2=skv.y&0xffff,s3=skv.y>>16;
  const unsigned c10=s1|(s0<<16), c32=s3|(s2<<16), c0z=s0, c21=s2|(s1<<16), cz3=s3<<16;
  float u[16];
  for(int m=0;m<8;m++){
    float e=dot2(kp[m+2],c0z,dot2(kp[m+1],c21,dot2(kp[m],cz3,0.f)));
    float o=dot2(kp[m+2],c10,dot2(kp[m+1],c32,0.f));
    u[2*m]=e*blo(vp[m]); u[2*m+1]=o*bhi(vp[m]); }
  v4u o0,o1; for(int m=0;m<4;m++){ o0[m]=cvtpk(u[15-2*m],u[14-2*m]); o1[m]=cvtpk(u[7-2*m],u[6-2*m]); }
  *(v4u*)(O+l*128+(((6-2*w)^(l&7))*16))=o0; *(v4u*)(O+l*128+(((7-2*w)^(l&7))*16))=o1;
  __syncthreads();
  for(int j=0;j<2;j++){ int d=(w*2+j)*8+(l>>3); v4u t=*(const v4u*)(O+d*128+(((l&7)^(d&7))*16));
    *(v4u*)(ws+WS_UT+(size_t)(db*64+d)*4096+(31-sb)*128+(l&7)*16)=t; }
}

DI void gemm1(int mb,int nb,const char* x,const char* w1,const u16* b1,char* ws,char* sm){
  const int tid=threadIdx.x, l=tid&63, w=__builtin_amdgcn_readfirstlane(tid>>6);
  const int wf=w>>1, wsx=w&1, hh=l>>5;
  auto stage=[&](int s){
    char* base=sm+(s&3)*32768;
    #pragma unroll
    for(int j=0;j<4;j++){ int p=w*4+j, r=p*8+(l>>3); int ch=(l&7)^((r>>1)&7);
      glds<AUXW>(x+(size_t)(mb*128+r)*1024+s*128+ch*16, base+p*1024); }
    #pragma unroll
    for(int j=0;j<4;j++){ int p=w*4+j, r=p*4+(l>>4); int ch=(l&15)^((l>>4)<<2);
      glds<AUXW>(w1+(size_t)(s*64+r)*3584+nb*256+ch*16, base+16384+p*1024); }
  };
  stage(0);stage(1);stage(2);
  f32x16 acc[2][2]={};
  const int qq=(l&15)>>2,p=l&3,blk=(l>>4)&1;
  int wa[2]; for(int c=0;c<2;c++) wa[c]=16384+256*(8*hh+qq)+64*((2*wf+c)^qq)+32*blk+8*p;
  const int xrow=wsx*64+(l&31), xsw=(xrow>>1)&7;
  for(int s=0;s<8;s++){
    if(s<6) asm volatile("s_waitcnt vmcnt(16)":::"memory"); else if(s==6) asm volatile("s_waitcnt vmcnt(8)":::"memory"); else asm volatile("s_waitcnt vmcnt(0)":::"memory");
    asm volatile("s_waitcnt lgkmcnt(0)":::"memory");
    __builtin_amdgcn_s_barrier();
    if(s+3<8) stage(s+3);
    const char* base=sm+(s&3)*32768;
    #pragma unroll
    for(int ks=0;ks<4;ks++){
      v4u A[2],B[2];
      for(int c=0;c<2;c++){ v2u t0=trd(base+wa[c]+4096*ks), t1=trd(base+wa[c]+4096*ks+1024); A[c]=v4u{t0.x,t0.y,t1.x,t1.y}; }
      for(int c=0;c<2;c++) B[c]=*(const v4u*)(base+(xrow+32*c)*128+(((2*ks+hh)^xsw)*16));
      for(int i=0;i<2;i++)for(int j=0;j<2;j++) acc[i][j]=mfma32(A[i],B[j],acc[i][j]);
    }
  }
  const float* cs=(const float*)(ws+WS_CS);
  for(int j=0;j<2;j++){
    const int srow=mb*128+wsx*64+32*j+(l&31);
    f32x4 st=*(const f32x4*)(ws+WS_ST+srow*16);
    const float r=st[2], c=st[0]*r;
    for(int i=0;i<2;i++){
      const int f0=nb*128+wf*64+32*i;
      float g[16];
      for(int rq=0;rq<4;rq++){
        f32x4 cv=*(const f32x4*)(cs+f0+8*rq+4*hh); v2u bv=*(const v2u*)(b1+f0+8*rq+4*hh);
        float bb[4]={blo(bv.x),bhi(bv.x),blo(bv.y),bhi(bv.y)};
        for(int e=0;e<4;e++){ float z=r*acc[i][j][4*rq+e]+(bb[e]-c*cv[e]);
          float t=z*(-2.302208198f-0.1029432397f*z*z);
          g[4*rq+e]=z*__builtin_amdgcn_rcpf(1.f+__builtin_amdgcn_exp2f(t)); }
      }
      const int sig=mb*4+wsx*2+j, kap=(f0>>4);
      for(int h2=0;h2<2;h2++){ v4u o; for(int e=0;e<4;e++) o[e]=cvtpk(g[8*h2+2*e],g[8*h2+2*e+1]);
        *(v4u*)(ws+WS_H+(size_t)(sig*112+kap+h2)*1024+l*16)=o; }
    }
  }
}

constexpr int CST=4512, CH_BYTES=4*CST+4224;
DI void conv(int c,const char* hk,const u16* sD,char* ws,char* sm){
  const int tid=threadIdx.x, l=tid&63, w=__builtin_amdgcn_readfirstlane(tid>>6);
  const int ch=w>>1, hf=w&1, d=2*c+ch;
  char* U=sm+ch*CH_BYTES; char* H=U+4*CST; char* X=sm+2*CH_BYTES;
  for(int j=0;j<2;j++) glds<AUX1>(hk+(size_t)d*4096+(hf*2+j)*1024+l*16, H+128+(hf*2+j)*1024);
  const __amdgpu_buffer_rsrc_t ru=__builtin_amdgcn_make_buffer_rsrc((void*)(ws+WS_UT+(size_t)d*4096),0,4096,0x00020000);
  for(int j=0;j<2;j++){ int o=(hf*2+j)*1024+l*16;
    v4u a=__builtin_amdgcn_raw_buffer_load_b128(ru,o,0,0), bq=__builtin_amdgcn_raw_buffer_load_b128(ru,o+8,0,0);
    unsigned dd[6]={a.x,a.y,a.z,a.w,bq.z,bq.w}; unsigned e[5]; for(int i=0;i<5;i++) e[i]=__builtin_amdgcn_alignbit(dd[i+1],dd[i],16);
    *(v4u*)(U+o)=a; *(v4u*)(U+CST+o)=v4u{e[0],e[1],e[2],e[3]}; *(v4u*)(U+2*CST+o)=v4u{dd[1],dd[2],dd[3],dd[4]}; *(v4u*)(U+3*CST+o)=v4u{e[1],e[2],e[3],e[4]}; }
  if(hf==0){ if(l<48){ for(int t=0;t<2;t++){ int id=l*2+t; *(v4u*)(U+(id/24)*CST+4096+(id%24)*16)=v4u{0,0,0,0}; } } }
  else if(l<8) *(v4u*)(H+l*16)=v4u{0,0,0,0};
  __syncthreads();
  const int row=l&15,g=l>>4;
  const char* ua=U+(3-(row&3))*CST+2*(1984-64*(row>>2)+8*g);
  const char* ha=H+16*g+8*row+8;
  f32x4 acc[8]={};
  #pragma unroll 1
  for(int r=4*hf;r<4*hf+4;r++){
    v4u A[8],B[8];
    for(int j=0;j<8;j++) A[j]=*(const v4u*)(ua+64*r-512*j);
    for(int i=0;i<8;i++){ v2u t0=*(const v2u*)(ha+64*r+512*i), t1=*(const v2u*)(ha+64*r+512*i+8); B[i]=v4u{t0.x,t0.y,t1.x,t1.y}; }
    #pragma unroll
    for(int i=0;i<8;i++)
      #pragma unroll
      for(int j=0;j<8;j++) if(i+j<8) acc[i+j]=mfma16(A[j],B[i],acc[i+j]);
  }
  f32x4 mine[4];
  if(hf){ for(int t=0;t<4;t++){ *(f32x4*)(X+(w*4+t)*1024+l*16)=acc[t]; mine[t]=acc[4+t]; } }
  else  { for(int t=0;t<4;t++){ *(f32x4*)(X+(w*4+t)*1024+l*16)=acc[4+t]; mine[t]=acc[t]; } }
  __syncthreads();
  const float Dv=blo(sD[d]);
  for(int t=0;t<4;t++){ f32x4 o=*(const f32x4*)(X+((w^1)*4+t)*1024+l*16); f32x4 y=mine[t]+o;
    int s0=256*(4*hf+t)+64*g+4*row; v2u uu=*(const v2u*)(U+2*(2044-s0));
    y[0]+=Dv*bhi(uu.y); y[1]+=Dv*blo(uu.y); y[2]+=Dv*bhi(uu.x); y[3]+=Dv*blo(uu.x);
    *(v2u*)(ws+WS_YT+(size_t)d*4096+s0*2)=v2u{cvtpk(y[0],y[1]),cvtpk(y[2],y[3])}; }
}
__global__ __launch_bounds__(256) void kB(const char* x,const char* w1,const u16* b1,const char* hk,const u16* sD,char* ws){
  extern __shared__ __attribute__((aligned(16))) char sm[];
  const int g=blockIdx.x&7,i=blockIdx.x>>3;
  if(i<28){ gemm1((g>>1)*4+(i&3),(g&1)*7+(i>>2),x,w1,b1,ws,sm); __syncthreads(); }
  conv(blockIdx.x,hk,sD,ws,sm);
}

__global__ __launch_bounds__(256) void kC(const char* x,const char* w2,const u16* b2,char* ws,char* out){
  extern __shared__ __attribute__((aligned(16))) char sm[];
  const int tid=threadIdx.x, l=tid&63, w=__builtin_amdgcn_readfirstlane(tid>>6);
  const int g=blockIdx.x&7,i=blockIdx.x>>3, mb=g*4+(i&3), nb=i>>2;
  const int hh=l>>5,qq=(l&15)>>2,p=l&3,blk=(l>>4)&1;
  char* W=sm+w*28672;
  const int srcch=(l&7)^(((l>>4)&1)<<2);
  const char* wsrc=w2+(size_t)(w*448+(l>>3))*1024+nb*128+srcch*16;
  const char* hsrc=ws+WS_H+(size_t)(mb*2*112+w*28)*1024+l*16;
  f32x16 acc[2][2]={};
  int wa[2]; for(int c=0;c<2;c++) wa[c]=128*(4*hh+qq)+64*(c^(qq>>1))+32*blk+8*p;
  for(int half=0;half<2;half++){
    asm volatile("s_waitcnt lgkmcnt(0)":::"memory");
    #pragma unroll
    for(int j=0;j<28;j++) glds<AUXW>(wsrc+(size_t)(half*28+j)*8192, W+j*1024);
    v4u B[14][2];
    #pragma unroll
    for(int kk=0;kk<14;kk++) for(int j=0;j<2;j++) B[kk][j]=*(const v4u*)(hsrc+(size_t)(j*112+half*14+kk)*1024);
    asm volatile("s_waitcnt vmcnt(0)":::"memory");
    #pragma unroll
    for(int kk=0;kk<14;kk++){
      v4u A[2]; for(int c=0;c<2;c++){ v2u t0=trd(W+wa[c]+2048*kk), t1=trd(W+wa[c]+2048*kk+1024); A[c]=v4u{t0.x,t0.y,t1.x,t1.y}; }
      for(int a=0;a<2;a++)for(int j=0;j<2;j++) acc[a][j]=mfma32(A[a],B[kk][j],acc[a][j]);
    }
  }
  __syncthreads();
  char* P=sm; char* Y=sm+4*64*272;
  for(int a=0;a<2;a++)for(int j=0;j<2;j++)for(int rq=0;rq<4;rq++){
    f32x4 t={acc[a][j][4*rq],acc[a][j][4*rq+1],acc[a][j][4*rq+2],acc[a][j][4*rq+3]};
    *(f32x4*)(P+(w*64+32*j+(l&31))*272+(32*a+8*rq+4*hh)*4)=t; }
  for(int j=0;j<2;j++){ int dd=(w*2+j)*8+(l>>3); *(v4u*)(Y+dd*144+(l&7)*16)=*(const v4u*)(ws+WS_YT+(size_t)(nb*64+dd)*4096+mb*128+(l&7)*16); }
  __syncthreads();
  for(int j=0;j<2;j++){
    const int s=(tid>>3)+32*j, d0=(tid&7)*8, srow=mb*64+s;
    float a[8]={};
    for(int ww=0;ww<4;ww++){ f32x4 t0=*(const f32x4*)(P+(ww*64+s)*272+d0*4), t1=*(const f32x4*)(P+(ww*64+s)*272+d0*4+16); for(int e=0;e<4;e++){a[e]+=t0[e];a[4+e]+=t1[e];} }
    v4u xv=*(const v4u*)(x+(size_t)srow*1024+nb*128+d0*2); v4u bv=*(const v4u*)((const char*)b2+nb*128+d0*2);
    f32x4 st=*(const f32x4*)(ws+WS_ST+srow*16);
    float o[8];
    for(int e=0;e<8;e++){ float xx=(e&1)?bhi(xv[e>>1]):blo(xv[e>>1]); float bb=(e&1)?bhi(bv[e>>1]):blo(bv[e>>1]);
      float y=blo(*(const u16*)(Y+(d0+e)*144+s*2)); o[e]=xx+(xx-st[0])*st[1]*y+a[e]+bb; }
    *(v4u*)(out+(size_t)srow*1024+nb*128+d0*2)=v4u{cvtpk(o[0],o[1]),cvtpk(o[2],o[3]),cvtpk(o[4],o[5]),cvtpk(o[6],o[7])};
  }
}

extern "C" void kernel_launch(void* const* d_in, const int* in_sizes, int n_in, void* d_out, int out_size, void* d_ws, size_t ws_size, hipStream_t stream){
  const char* q=(const char*)d_in[0],*k=(const char*)d_in[1],*v=(const char*)d_in[2]; const u16* sk=(const u16*)d_in[3]; const char* hk=(const char*)d_in[4]; const u16* sD=(const u16*)d_in[5];
  const char* w1=(const char*)d_in[7]; const u16* b1=(const u16*)d_in[8]; const char* w2=(const char*)d_in[9]; const u16* b2=(const u16*)d_in[10];
  char* ws=(char*)d_ws;
  (void)hipFuncSetAttribute((const void*)kB,hipFuncAttributeMaxDynamicSharedMemorySize,131072);
  (void)hipFuncSetAttribute((const void*)kC,hipFuncAttributeMaxDynamicSharedMemorySize,131072);
  kA<<<256+28,256,0,stream>>>(q,k,v,sk,w1,ws);
  kB<<<256,256,131072,stream>>>(q,w1,b1,hk,sD,ws);
  kC<<<256,256,131072,stream>>>(q,w2,b2,ws,(char*)d_out);
}
```

```cpp
#include <hip/hip_runtime.h>
#include <hip/hip_bf16.h>
typedef unsigned short u16;
typedef unsigned v4u __attribute__((ext_vector_type(4)));
typedef unsigned v2u __attribute__((ext_vector_type(2)));
typedef float f32x16 __attribute__((ext_vector_type(16)));
typedef float f32x4 __attribute__((ext_vector_type(4)));
typedef float f32x2 __attribute__((ext_vector_type(2)));
typedef short s16x4 __attribute__((ext_vector_type(4)));
typedef __bf16 bf16x8 __attribute__((ext_vector_type(8)));
typedef __bf16 bf16x2 __attribute__((ext_vector_type(2)));
#define GAS __attribute__((address_space(1)))
#define LAS __attribute__((address_space(3)))
#define DI __device__ __forceinline__
constexpr int S=2048, D=512, F=1792;
#define AUX1 0
#define AUXW 0
constexpr size_t WS_UT=0, WS_YT=2u<<20, WS_H=4u<<20, WS_ST=12u<<20, WS_CS=WS_ST+65536;

DI unsigned cvtpk(float lo,float hi){ f32x2 v={lo,hi}; return __builtin_bit_cast(unsigned,__builtin_convertvector(v,bf16x2)); }
DI float blo(unsigned x){ return __uint_as_float(x<<16); }
DI float bhi(unsigned x){ return __uint_as_float(x&0xffff0000u); }
DI float dot2(unsigned a,unsigned b,float c){ return __builtin_amdgcn_fdot2_f32_bf16(__builtin_bit_cast(bf16x2,a),__builtin_bit_cast(bf16x2,b),c,false); }
DI v2u trd(const char* p){ return __builtin_bit_cast(v2u,__builtin_amdgcn_ds_read_tr16_b64_v4i16((LAS s16x4*)p)); }
DI f32x16 mfma32(v4u a,v4u b,f32x16 c){ return __builtin_amdgcn_mfma_f32_32x32x16_bf16(__builtin_bit_cast(bf16x8,a),__builtin_bit_cast(bf16x8,b),c,0,0,0); }
DI f32x4 mfma16(v4u a,v4u b,f32x4 c){ return __builtin_amdgcn_mfma_f32_16x16x32_bf16(__builtin_bit_cast(bf16x8,a),__builtin_bit_cast(bf16x8,b),c,0,0,0); }
template<int AUX> DI void glds(const void* g, char* l){ __builtin_amdgcn_global_load_lds((const GAS void*)g,(LAS void*)l,16,0,AUX); }
DI float wsum(float a){ for(int o=32;o;o>>=1) a+=__shfl_xor(a,o); return a; }

__global__ __launch_bounds__(256) void kA(const char* q,const char* k,const char* v,const u16* sk,const char* w1,char* ws){
  __shared__ __attribute__((aligned(16))) char sm[9216+8192+8192+1024*8];
  const int tid=threadIdx.x, l=tid&63, w=__builtin_amdgcn_readfirstlane(tid>>6), b=blockIdx.x;
  if(b>=256){
    int cb=b-256; float a[8]={};
    for(int i=0;i<16;i++){ int r=i*32+(tid>>3); v4u x=*(const v4u*)(w1+(size_t)r*3584+cb*128+(tid&7)*16);
      for(int j=0;j<4;j++){ a[2*j]+=blo(x[j]); a[2*j+1]+=bhi(x[j]); } }
    float* red=(float*)sm;
    for(int j=0;j<8;j++) red[(tid>>3)*64+(tid&7)*8+j]=a[j];
    __syncthreads();
    if(tid<64){ float s=0; for(int i=0;i<32;i++) s+=red[i*64+tid]; ((float*)(ws+WS_CS))[cb*64+tid]=s; }
    return;
  }
  const int sb=b>>3, db=b&7;
  char* K=sm; char* V=sm+9216; char* O=sm+9216+8192;
  const __amdgpu_buffer_rsrc_t rk=__builtin_amdgcn_make_buffer_rsrc((void*)k,0,S*D*2,0x00020000);
  const int srcch=(l&7)^(((l>>4)&1)<<2);
  for(int p=w;p<9;p+=4){ int r=sb*64-8+p*8+(l>>3);
    __builtin_amdgcn_raw_ptr_buffer_load_lds(rk,(LAS void*)(K+p*1024),16,r*1024+db*128+srcch*16,0,0,AUX1); }
  for(int p=w;p<8;p+=4){ int r=sb*64+p*8+(l>>3); glds<AUX1>(v+(size_t)r*1024+db*128+srcch*16,V+p*1024); }
  v4u xr[2]; for(int j=0;j<2;j++) xr[j]=*(const v4u*)(q+(size_t)(b*8+w*2+j)*1024+l*16);
  v2u skv=*(const v2u*)(sk+(db*64+l)*4);
  for(int j=0;j<2;j++){ float s1=0,s2=0; for(int e=0;e<4;e++){ s1=dot2(xr[j][e],0x3f803f80u,s1); s2=dot2(xr[j][e],xr[j][e],s2);}
    s1=wsum(s1); s2=wsum(s2); float m=s1*(1.f/D); float var=fmaxf(s2*(1.f/D)-m*m,0.f);
    float r1=rsqrtf(var+1e-5f); float var2=var*r1*r1; float r=r1*rsqrtf(var2+1e-5f);
    if(l==0) *(f32x4*)(ws+WS_ST+(b*8+w*2+j)*16)=f32x4{m,r1,r,0.f}; }
  __syncthreads();
  const int G=l>>4,qq=(l&15)>>2,p=l&3;
  const int la=qq*128+((((G>>1)^(qq>>1))&1)*64)+(G&1)*32+p*8;
  unsigned kp[10],vp[8];
  for(int i=0;i<5;i++){ v2u t=trd(K+(4+16*w+4*i)*128+la); kp[2*i]=t.x; kp[2*i+1]=t.y; }
  for(int i=0;i<4;i++){ v2u t=trd(V+(16*w+4*i)*128+la); vp[2*i]=t.x; vp[2*i+1]=t.y; }
  const unsigned s0=skv.x&0xffff,s1=skv.x>>16,s2=skv.y&0xffff,s3=skv.y>>16;
  const unsigned c10=s1|(s0<<16), c32=s3|(s2<<16), c0z=s0, c21=s2|(s1<<16), cz3=s3<<16;
  float u[16];
  for(int m=0;m<8;m++){
    float e=dot2(kp[m+2],c0z,dot2(kp[m+1],c21,dot2(kp[m],cz3,0.f)));
    float o=dot2(kp[m+2],c10,dot2(kp[m+1],c32,0.f));
    u[2*m]=e*blo(vp[m]); u[2*m+1]=o*bhi(vp[m]); }
  v4u o0,o1; for(int m=0;m<4;m++){ o0[m]=cvtpk(u[15-2*m],u[14-2*m]); o1[m]=cvtpk(u[7-2*m],u[6-2*m]); }
  *(v4u*)(O+l*128+(((6-2*w)^(l&7))*16))=o0; *(v4u*)(O+l*128+(((7-2*w)^(l&7))*16))=o1;
  __syncthreads();
  for(int j=0;j<2;j++){ int d=(w*2+j)*8+(l>>3); v4u t=*(const v4u*)(O+d*128+(((l&7)^(d&7))*16));
    *(v4u*)(ws+WS_UT+(size_t)(db*64+d)*4096+(31-sb)*128+(l&7)*16)=t; }
}

DI void gemm1(int mb,int nb,const char* x,const char* w1,const u16* b1,char* ws,char* sm){
  const int tid=threadIdx.x, l=tid&63, w=__builtin_amdgcn_readfirstlane(tid>>6);
  const int wf=w>>1, wsx=w&1, hh=l>>5;
  auto stage=[&](int s){
    char* base=sm+(s&3)*32768;
    #pragma unroll
    for(int j=0;j<4;j++){ int p=w*4+j, r=p*8+(l>>3); int ch=(l&7)^((r>>1)&7);
      glds<AUXW>(x+(size_t)(mb*128+r)*1024+s*128+ch*16, base+p*1024); }
    #pragma unroll
    for(int j=0;j<4;j++){ int p=w*4+j, r=p*4+(l>>4); int ch=(l&15)^((l>>4)<<2);
      glds<AUXW>(w1+(size_t)(s*64+r)*3584+nb*256+ch*16, base+16384+p*1024); }
  };
  stage(0);stage(1);stage(2);
  f32x16 acc[2][2]={};
  const int qq=(l&15)>>2,p=l&3,blk=(l>>4)&1;
  int wa[2]; for(int c=0;c<2;c++) wa[c]=16384+256*(8*hh+qq)+64*((2*wf+c)^qq)+32*blk+8*p;
  const int xrow=wsx*64+(l&31), xsw=(xrow>>1)&7;
  for(int s=0;s<8;s++){
    if(s<6) asm volatile("s_waitcnt vmcnt(16)":::"memory"); else if(s==6) asm volatile("s_waitcnt vmcnt(8)":::"memory"); else asm volatile("s_waitcnt vmcnt(0)":::"memory");
    asm volatile("s_waitcnt lgkmcnt(0)":::"memory");
    __builtin_amdgcn_s_barrier();
    if(s+3<8) stage(s+3);
    const char* base=sm+(s&3)*32768;
    #pragma unroll
    for(int ks=0;ks<4;ks++){
      v4u A[2],B[2];
      for(int c=0;c<2;c++){ v2u t0=trd(base+wa[c]+4096*ks), t1=trd(base+wa[c]+4096*ks+1024); A[c]=v4u{t0.x,t0.y,t1.x,t1.y}; }
      for(int c=0;c<2;c++) B[c]=*(const v4u*)(base+(xrow+32*c)*128+(((2*ks+hh)^xsw)*16));
      for(int i=0;i<2;i++)for(int j=0;j<2;j++) acc[i][j]=mfma32(A[i],B[j],acc[i][j]);
    }
  }
  const float* cs=(const float*)(ws+WS_CS);
  for(int j=0;j<2;j++){
    const int srow=mb*128+wsx*64+32*j+(l&31);
    f32x4 st=*(const f32x4*)(ws+WS_ST+srow*16);
    const float r=st[2], c=st[0]*r;
    for(int i=0;i<2;i++){
      const int f0=nb*128+wf*64+32*i;
      float g[16];
      for(int rq=0;rq<4;rq++){
        f32x4 cv=*(const f32x4*)(cs+f0+8*rq+4*hh); v2u bv=*(const v2u*)(b1+f0+8*rq+4*hh);
        float bb[4]={blo(bv.x),bhi(bv.x),blo(bv.y),bhi(bv.y)};
        for(int e=0;e<4;e++){ float z=r*acc[i][j][4*rq+e]+(bb[e]-c*cv[e]);
          float t=z*(-2.302208198f-0.1029432397f*z*z);
          g[4*rq+e]=z*__builtin_amdgcn_rcpf(1.f+__builtin_amdgcn_exp2f(t)); }
      }
      const int sig=mb*4+wsx*2+j, kap=(f0>>4);
      for(int h2=0;h2<2;h2++){ v4u o; for(int e=0;e<4;e++) o[e]=cvtpk(g[8*h2+2*e],g[8*h2+2*e+1]);
        *(v4u*)(ws+WS_H+(size_t)(sig*112+kap+h2)*1024+l*16)=o; }
    }
  }
}

constexpr int CST=4512, CH_BYTES=4*CST+4224;
DI void conv(int c,const char* hk,const u16* sD,char* ws,char* sm){
  const int tid=threadIdx.x, l=tid&63, w=__builtin_amdgcn_readfirstlane(tid>>6);
  const int ch=w>>1, hf=w&1, d=2*c+ch;
  char* U=sm+ch*CH_BYTES; char* H=U+4*CST; char* X=sm+2*CH_BYTES;
  for(int j=0;j<2;j++) glds<AUX1>(hk+(size_t)d*4096+(hf*2+j)*1024+l*16, H+128+(hf*2+j)*1024);
  const __amdgpu_buffer_rsrc_t ru=__builtin_amdgcn_make_buffer_rsrc((void*)(ws+WS_UT+(size_t)d*4096),0,4096,0x00020000);
  for(int j=0;j<2;j++){ int o=(hf*2+j)*1024+l*16;
    v4u a=__builtin_amdgcn_raw_buffer_load_b128(ru,o,0,0), bq=__builtin_amdgcn_raw_buffer_load_b128(ru,o+8,0,0);
    unsigned dd[6]={a.x,a.y,a.z,a.w,bq.z,bq.w}; unsigned e[5]; for(int i=0;i<5;i++) e[i]=__builtin_amdgcn_alignbit(dd[i+1],dd[i],16);
    *(v4u*)(U+o)=a; *(v4u*)(U+CST+o)=v4u{e[0],e[1],e[2],e[3]}; *(v4u*)(U+2*CST+o)=v4u{dd[1],dd[2],dd[3],dd[4]}; *(v4u*)(U+3*CST+o)=v4u{e[1],e[2],e[3],e[4]}; }
  if(hf==0){ if(l<48){ for(int t=0;t<2;t++){ int id=l*2+t; *(v4u*)(U+(id/24)*CST+4096+(id%24)*16)=v4u{0,0,0,0}; } } }
  else if(l<8) *(v4u*)(H+l*16)=v4u{0,0,0,0};
  __syncthreads();
  const int row=l&15,g=l>>4;
  const char* ua=U+(3-(row&3))*CST+2*(1984-64*(row>>2)+8*g);
  const char* ha=H+16*g+8*row+8;
  f32x4 acc[8]={};
  #pragma unroll 1
  for(int r=4*hf;r<4*hf+4;r++){
    v4u A[8],B[8];
    for(int j=0;j<8;j++) A[j]=*(const v4u*)(ua+64*r-512*j);
    for(int i=0;i<8;i++){ v2u t0=*(const v2u*)(ha+64*r+512*i), t1=*(const v2u*)(ha+64*r+512*i+8); B[i]=v4u{t0.x,t0.y,t1.x,t1.y}; }
    #pragma unroll
    for(int i=0;i<8;i++)
      #pragma unroll
      for(int j=0;j<8;j++) if(i+j<8) acc[i+j]=mfma16(A[j],B[i],acc[i+j]);
  }
  f32x4 mine[4];
  if(hf){ for(int t=0;t<4;t++){ *(f32x4*)(X+(w*4+t)*1024+l*16)=acc[t]; mine[t]=acc[4+t]; } }
  else  { for(int t=0;t<4;t++){ *(f32x4*)(X+(w*4+t)*1024+l*16)=acc[4+t]; mine[t]=acc[t]; } }
  __syncthreads();
  const float Dv=blo(sD[d]);
  for(int t=0;t<4;t++){ f32x4 o=*(const f32x4*)(X+((w^1)*4+t)*1024+l*16); f32x4 y=mine[t]+o;
    int s0=256*(4*hf+t)+64*g+4*row; v2u uu=*(const v2u*)(U+2*(2044-s0));
    y[0]+=Dv*bhi(uu.y); y[1]+=Dv*blo(uu.y); y[2]+=Dv*bhi(uu.x); y[3]+=Dv*blo(uu.x);
    *(v2u*)(ws+WS_YT+(size_t)d*4096+s0*2)=v2u{cvtpk(y[0],y[1]),cvtpk(y[2],y[3])}; }
}
__global__ __launch_bounds__(256) void kB(const char* x,const char* w1,const u16* b1,const char* hk,const u16* sD,char* ws){
  extern __shared__ __attribute__((aligned(16))) char sm[];
  const int g=blockIdx.x&7,i=blockIdx.x>>3;
  if(i<28){ gemm1((g>>1)*4+(i&3),(g&1)*7+(i>>2),x,w1,b1,ws,sm); __syncthreads(); }
  conv(blockIdx.x,hk,sD,ws,sm);
}

__global__ __launch_bounds__(256) void kC(const char* x,const char* w2,const u16* b2,char* ws,char* out){
  extern __shared__ __attribute__((aligned(16))) char sm[];
  const int tid=threadIdx.x, l=tid&63, w=__builtin_amdgcn_readfirstlane(tid>>6);
  const int g=blockIdx.x&7,i=blockIdx.x>>3, mb=g*4+(i&3), nb=i>>2;
  const int hh=l>>5,qq=(l&15)>>2,p=l&3,blk=(l>>4)&1;
  char* W=sm+w*28672;
  const int srcch=(l&7)^(((l>>4)&1)<<2);
  const char* wsrc=w2+(size_t)(w*448+(l>>3))*1024+nb*128+srcch*16;
  const char* hsrc=ws+WS_H+(size_t)(mb*2*112+w*28)*1024+l*16;
  f32x16 acc[2][2]={};
  int wa[2]; for(int c=0;c<2;c++) wa[c]=128*(4*hh+qq)+64*(c^(qq>>1))+32*blk+8*p;
  for(int half=0;half<2;half++){
    asm volatile("s_waitcnt lgkmcnt(0)":::"memory");
    #pragma unroll
    for(int j=0;j<28;j++) glds<AUXW>(wsrc+(size_t)(half*28+j)*8192, W+j*1024);
    v4u B[14][2];
    #pragma unroll
    for(int kk=0;kk<14;kk++) for(int j=0;j<2;j++) B[kk][j]=*(const v4u*)(hsrc+(size_t)(j*112+half*14+kk)*1024);
    asm volatile("s_waitcnt vmcnt(0)":::"memory");
    #pragma unroll
    for(int kk=0;kk<14;kk++){
      v4u A[2]; for(int c=0;c<2;c++){ v2u t0=trd(W+wa[c]+2048*kk), t1=trd(W+wa[c]+2048*kk+1024); A[c]=v4u{t0.x,t0.y,t1.x,t1.y}; }
      for(int a=0;a<2;a++)for(int j=0;j<2;j++) acc[a][j]=mfma32(A[a],B[kk][j],acc[a][j]);
    }
  }
  __syncthreads();
  char* P=sm; char* Y=sm+4*64*272;
  for(int a=0;a<2;a++)for(int j=0;j<2;j++)for(int rq=0;rq<4;rq++){
    f32x4 t={acc[a][j][4*rq],acc[a][j][4*rq+1],acc[a][j][4*rq+2],acc[a][j][4*rq+3]};
    *(f32x4*)(P+(w*64+32*j+(l&31))*272+(32*a+8*rq+4*hh)*4)=t; }
  for(int j=0;j<2;j++){ int dd=(w*2+j)*8+(l>>3); *(v4u*)(Y+dd*144+(l&7)*16)=*(const v4u*)(ws+WS_YT+(size_t)(nb*64+dd)*4096+mb*128+(l&7)*16); }
  __syncthreads();
  for(int j=0;j<2;j++){
    const int s=(tid>>3)+32*j, d0=(tid&7)*8, srow=mb*64+s;
    float a[8]={};
    for(int ww=0;ww<4;ww++){ f32x4 t0=*(const f32x4*)(P+(ww*64+s)*272+d0*4), t1=*(const f32x4*)(P+(ww*64+s)*272+d0*4+16); for(int e=0;e<4;e++){a[e]+=t0[e];a[4+e]+=t1[e];} }
    v4u xv=*(const v4u*)(x+(size_t)srow*1024+nb*128+d0*2); v4u bv=*(const v4u*)((const char*)b2+nb*128+d0*2);
    f32x4 st=*(const f32x4*)(ws+WS_ST+srow*16);
    float o[8];
    for(int e=0;e<8;e++){ float xx=(e&1)?bhi(xv[e>>1]):blo(xv[e>>1]); float bb=(e&1)?bhi(bv[e>>1]):blo(bv[e>>1]);
      float y=blo(*(const u16*)(Y+(d0+e)*144+s*2)); o[e]=xx+(xx-st[0])*st[1]*y+a[e]+bb; }
    *(v4u*)(out+(size_t)srow*1024+nb*128+d0*2)=v4u{cvtpk(o[0],o[1]),cvtpk(o[2],o[3]),cvtpk(o[4],o[5]),cvtpk(o[6],o[7])};
  }
}

__global__ void kE(const char* bx,char* ws){ unsigned o=threadIdx.x*4,acc=0;
#pragma unroll 1
for(int i=0;i<64;i++){ unsigned v; asm volatile("global_load_dword %0, %1, %2\n\ts_waitcnt vmcnt(0)":"=&v"(v):"v"(o),"s"(bx):"memory"); acc+=v; o=(i+1)*32768-4096+threadIdx.x*4+(v==0x12345678u?64:0); if(o>=2097152-1024)o=1024; } if(acc==0x7654321u) *(unsigned*)(ws+(100u<<20))=acc; }
extern "C" void kernel_launch(void* const* d_in, const int* in_sizes, int n_in, void* d_out, int out_size, void* d_ws, size_t ws_size, hipStream_t stream){
  const char* q=(const char*)d_in[0],*k=(const char*)d_in[1],*v=(const char*)d_in[2]; const u16* sk=(const u16*)d_in[3]; const char* hk=(const char*)d_in[4]; const u16* sD=(const u16*)d_in[5];
  const char* w1=(const char*)d_in[7]; const u16* b1=(const u16*)d_in[8]; const char* w2=(const char*)d_in[9]; const u16* b2=(const u16*)d_in[10];
  char* ws=(char*)d_ws;
  (void)hipFuncSetAttribute((const void*)kB,hipFuncAttributeMaxDynamicSharedMemorySize,131072);
  (void)hipFuncSetAttribute((const void*)kC,hipFuncAttributeMaxDynamicSharedMemorySize,131072);
  kE<<<8,64,0,stream>>>((const char*)d_in[6],ws);
  kA<<<256+28,256,0,stream>>>(q,k,v,sk,w1,ws);
  kB<<<256,256,131072,stream>>>(q,w1,b1,hk,sD,ws);
  kC<<<256,256,131072,stream>>>(q,w2,b2,ws,(char*)d_out);
}
```

```cpp
#include <hip/hip_runtime.h>
typedef unsigned u32;
__global__ __launch_bounds__(256, 1) void fused_kernel(
    const void* x, u32 xs, u32 xf, const void* w1, u32 w1s, u32 w1f, const void* k, u32 ks, u32 kf, const void* v, u32 vs, u32 vf,
    void* ws, u32 wss, u32 wsf, const void* w2, u32 w2s, u32 w2f, const void* hk, u32 hs, u32 hf, void* out, u32 os, u32 of,
    const void* sk, const void* b1, const void* sd, const void* b2) {
  asm volatile(
"LSTART:\n"
"s_load_dwordx16 s[8:23], %0, 0x0\n"
"s_load_dwordx16 s[24:39], %0, 0x40\n"
"s_load_dwordx8 s[40:47], %0, 0x80\n"
"s_mov_b32 s4, %1\n"
"s_getpc_b64 s[64:65]\n"
"LPC:\n"
"s_lshl_b32 s66, s4, 2\n"
"v_lshrrev_b32 v1, 3, v0\n"
"v_and_b32 v2, 7, v0\n"
"v_add3_u32 v1, s66, v1, v2\n"
"s_mov_b64 exec, 0xff\n"
"v_and_b32 v1, 0xff, v1\n"
"v_lshlrev_b32 v1, 7, v1\n"
"v_min_u32 v1, LFIN-LPC-4, v1\n"
"global_load_dword v47, v1, s[64:65]\n"
"s_mov_b64 exec, -1\n"
"v_lshrrev_b32 v1, 6, v0\n"
"v_and_b32 v0, 63, v0\n"
"s_and_b32 s6, s4, 7\n"
"v_readfirstlane_b32 s5, v1\n"
"s_lshr_b32 s7, s4, 3\n"
"v_lshlrev_b32 v1, 4, v0\n"
"v_lshrrev_b32 v227, 3, v0\n"
"v_and_b32 v228, 7, v0\n"
"v_lshrrev_b32 v229, 4, v0\n"
"v_bfe_u32 v230, v0, 2, 2\n"
"v_and_b32 v231, 3, v0\n"
"v_lshrrev_b32 v232, 5, v0\n"
"v_and_b32 v233, 1, v229\n"
"v_and_b32 v234, 31, v0\n"
"v_lshlrev_b32 v222, 2, v233\n"
"v_xor_b32 v222, v228, v222\n"
"v_lshlrev_b32 v223, 10, v227\n"
"v_lshl_or_b32 v2, v222, 4, v223\n"
"v_add_u32 v3, 7168, v2\n"
"v_add_u32 v4, 14336, v2\n"
"v_add_u32 v5, 21504, v2\n"
"s_lshl_b32 s59, s5, 12\n"
"s_lshl_b32 s78, s5, 13\n"
"s_lshl_b32 s49, s7, 16\n"
"s_lshl_b32 s50, s5, 14\n"
"s_add_u32 s49, s49, s50\n"
"s_lshl_b32 s50, s6, 7\n"
"s_add_u32 s52, s49, s50\n"
"s_sub_u32 s50, s52, 8192\n"
"v_add_u32 v215, s50, v2\n"
"s_mul_i32 s53, s5, 5120\n"
"s_add_u32 s53, s53, 131072\n"
"s_lshl_b32 s49, s4, 2\n"
"s_add_u32 s49, s49, s5\n"
"s_mul_i32 s54, s49, 1792\n"
"v_lshlrev_b32 v15, 7, v0\n"
"s_lshl_b32 s50, s6, 9\n"
"v_lshlrev_b32 v44, 3, v0\n"
"v_add_u32 v221, s50, v44\n"
"s_lshl_b32 s50, s6, 8\n"
"s_lshl_b32 s63, s7, 3\n"
"s_add_u32 s50, s50, s63\n"
"s_lshl_b32 s63, s5, 1\n"
"s_add_u32 s50, s50, s63\n"
"s_lshl_b32 s63, s50, 10\n"
"s_lshl_b32 s50, s50, 4\n"
"s_add_u32 s83, s50, 16777216\n"
"v_lshlrev_b32 v37, 4, v234\n"
"v_lshl_add_u32 v70, v232, 10, v37\n"
"v_add_u32 v70, s63, v70\n"
"s_waitcnt lgkmcnt(0)\n"
"buffer_load_dwordx4 v[76:79], v70, s[8:11], 0 offen\n"
"buffer_load_dwordx4 v[80:83], v70, s[8:11], 0 offen offset:512\n"
"s_mov_b32 s62, 17563648\n"
"buffer_load_dword v66, off, s[24:27], s62 sc1\n"
"s_cmp_ge_u32 s7, 28\n"
"s_cbranch_scc1 LHELP\n"
"s_lshr_b32 s50, s6, 1\n"
"s_mov_b32 m0, s53\n"
"global_load_dwordx2 v[162:163], v221, s[40:41]\n"
"s_lshl_b32 s50, s50, 2\n"
"s_and_b32 s56, s7, 3\n"
"s_add_u32 s56, s56, s50\n"
"s_mov_b32 exec_lo, 0\n"
"buffer_load_dwordx4 v215, s[16:19], 0 offen lds\n"
"s_mov_b32 exec_lo, -1\n"
"s_and_b32 s50, s6, 1\n"
"s_mul_i32 s50, s50, 7\n"
"s_lshr_b32 s57, s7, 2\n"
"s_add_u32 m0, s53, 1024\n"
"s_nop 0\n"
"buffer_load_dwordx4 v2, s[16:19], s52 offen lds\n"
"s_add_u32 s60, s7, s57\n"
"s_and_b32 s60, s60, 3\n"
"s_mov_b32 s55, 0x76532410\n"
"buffer_load_dwordx4 v3, s[16:19], s52 offen offset:1024 lds\n"
"s_cmp_eq_u32 s60, 1\n"
"s_cselect_b32 s55, 0x76435210, s55\n"
"s_cmp_eq_u32 s60, 2\n"
"s_cselect_b32 s55, 0x75463102, s55\n"
"s_add_u32 m0, s53, 3072\n"
"s_nop 0\n"
"buffer_load_dwordx4 v2, s[20:23], s52 offen lds\n"
"s_cmp_eq_u32 s60, 3\n"
"s_cselect_b32 s55, 0x65742130, s55\n"
"s_mov_b32 s58, 0\n"
"buffer_load_dwordx4 v3, s[20:23], s52 offen offset:1024 lds\n"
"s_lshl_b32 s58, s58, 7\n"
"s_add_u32 s57, s57, s50\n"
"s_lshl_b32 s50, s56, 17\n"
"s_lshl_b32 s60, s5, 15\n"
"s_add_u32 s50, s50, s60\n"
"v_add_u32 v224, s50, v223\n"
"v_xor_b32 v222, v228, v229\n"
"v_lshl_add_u32 v236, v222, 4, v224\n"
"v_xor_b32 v222, 4, v222\n"
"v_lshl_add_u32 v237, v222, 4, v224\n"
"v_add_u32 v238, 14336, v236\n"
"v_add_u32 v239, 21504, v237\n"
"v_add_u32 v237, 7168, v237\n"
"s_mul_i32 s50, s5, 57344\n"
"s_lshl_b32 s60, s57, 8\n"
"s_add_u32 s50, s50, s60\n"
"v_mul_u32_u24 v224, 3584, v229\n"
"v_and_b32 v225, 15, v0\n"
"v_lshlrev_b32 v222, 2, v229\n"
"v_xor_b32 v222, v225, v222\n"
"v_lshl_add_u32 v224, v222, 4, v224\n"
"v_add_u32 v240, s50, v224\n"
"v_add_u32 v241, 13312, v240\n"
"v_add_u32 v242, 26624, v240\n"
"v_add_u32 v243, 39936, v240\n"
"s_mov_b32 s50, 17563648\n"
"v_lshlrev_b32 v226, 5, v233\n"
"v_lshl_add_u32 v226, v231, 3, v226\n"
"v_lshrrev_b32 v222, 1, v230\n"
"v_xor_b32 v224, v232, v222\n"
"v_lshl_add_u32 v224, v224, 6, v226\n"
"v_lshl_add_u32 v224, v230, 7, v224\n"
"v_add_u32 v209, s53, v224\n"
"s_lshr_b32 s60, s5, 1\n"
"v_xor_b32 v222, s60, v222\n"
"v_lshl_add_u32 v224, v232, 2, v230\n"
"v_lshl_add_u32 v31, v224, 6, v226\n"
"v_lshl_add_u32 v224, v224, 7, v226\n"
"v_lshl_add_u32 v10, v222, 6, v224\n"
"v_lshl_add_u32 v224, v232, 3, v230\n"
"v_lshl_add_u32 v224, v224, 8, v226\n"
"s_and_b32 s60, s5, 2\n"
"v_xor_b32 v222, s60, v230\n"
"v_lshl_add_u32 v244, v222, 6, v224\n"
"v_add_u32 v244, 16384, v244\n"
"v_xor_b32 v245, 64, v244\n"
"v_add_u32 v246, 65536, v244\n"
"v_add_u32 v247, 65536, v245\n"
"s_and_b32 s61, s5, 1\n"
"s_lshl_b32 s60, s61, 13\n"
"v_lshlrev_b32 v224, 7, v234\n"
"v_add_u32 v224, s60, v224\n"
"v_bfe_u32 v223, v0, 1, 3\n"
"v_xor_b32 v222, v223, v232\n"
"v_lshl_add_u32 v248, v222, 4, v224\n"
"v_xor_b32 v249, 32, v248\n"
"v_xor_b32 v250, 64, v248\n"
"v_xor_b32 v251, 96, v248\n"
"v_add_u32 v252, 65536, v248\n"
"v_add_u32 v253, 65536, v249\n"
"v_add_u32 v254, 65536, v250\n"
"v_add_u32 v255, 65536, v251\n"
"s_lshl_b32 s62, s5, 1\n"
"s_bfe_u32 s60, s55, 0x30000\n"
"s_add_u32 m0, s59, 0\n"
"s_lshl_b32 s60, s60, 7\n"
"buffer_load_dwordx4 v236, s[8:11], s60 offen offset:0 lds\n"
"s_sub_u32 s62, 6, s62\n"
"v_xor_b32 v222, s62, v228\n"
"v_lshlrev_b32 v224, 7, v0\n"
"buffer_load_dwordx4 v237, s[8:11], s60 offen offset:1024 lds\n"
"v_lshl_add_u32 v210, v222, 4, v224\n"
"v_add_u32 v210, 151552, v210\n"
"v_xor_b32 v211, 16, v210\n"
"buffer_load_dwordx4 v238, s[8:11], s60 offen offset:2048 lds\n"
"v_xor_b32 v222, v228, v227\n"
"v_lshlrev_b32 v222, 4, v222\n"
"v_lshl_add_u32 v222, v227, 7, v222\n"
"buffer_load_dwordx4 v239, s[8:11], s60 offen offset:3072 lds\n"
"s_lshl_b32 s62, s5, 11\n"
"s_add_u32 s62, s62, 151552\n"
"v_add_u32 v212, s62, v222\n"
"s_add_u32 m0, s59, 16384\n"
"s_mul_i32 s60, s60, 1792\n"
"buffer_load_dwordx4 v240, s[12:15], s60 offen offset:0 lds\n"
"v_lshlrev_b32 v222, 4, v228\n"
"v_lshl_add_u32 v214, v227, 12, v222\n"
"s_lshl_b32 s79, s6, 18\n"
"s_lshl_b32 s62, s5, 16\n"
"s_add_u32 s79, s79, s62\n"
"s_lshl_b32 s62, s7, 7\n"
"s_lshr_b32 s54, s79, 4\n"
"s_add_u32 s54, s54, s62\n"
"s_sub_u32 s79, s79, s62\n"
"s_add_u32 s79, s79, 8392576\n"
"v_add_u32 v6, s78, v10\n"
"v_xor_b32 v7, 64, v6\n"
"v_add_u32 v8, 65536, v6\n"
"v_add_u32 v9, 65536, v7\n"
"s_lshl_b32 s62, s61, 10\n"
"v_add_u32 v16, s62, v1\n"
"v_xor_b32 v17, 1024, v16\n"
"v_lshlrev_b32 v42, 3, v234\n"
"s_add_u32 s62, s78, 32768\n"
"v_add_u32 v32, s62, v1\n"
"v_xor_b32 v33, 8192, v32\n"
"v_xor_b32 v34, 16384, v32\n"
"v_xor_b32 v35, 24576, v32\n"
"s_mov_b32 s62, s59\n"
"v_add_u32 v30, s62, v1\n"
"v_add_u32 v31, s62, v31\n"
"v_lshlrev_b32 v222, 6, v234\n"
"v_lshl_add_u32 v222, v230, 4, v222\n"
"v_lshl_add_u32 v222, v232, 3, v222\n"
"v_add_u32 v26, s62, v222\n"
"v_xor_b32 v27, 16, v26\n"
"v_xor_b32 v28, 32, v26\n"
"v_xor_b32 v29, 48, v26\n"
"v_lshrrev_b32 v222, 2, v0\n"
"v_lshlrev_b32 v224, 4, v231\n"
"v_lshl_add_u32 v25, v222, 12, v224\n"
"v_lshlrev_b32 v222, 10, v222\n"
"v_xor_b32 v224, v231, v229\n"
"v_lshl_add_u32 v23, v224, 4, v222\n"
"v_add_u32 v24, 15360, v23\n"
"s_lshr_b32 s51, s5, 1\n"
"s_bfe_u32 s50, s4, 0x20005\n"
"s_cmp_eq_u32 s50, 3\n"
"s_cselect_b32 s50, 128, 0\n"
"s_xor_b32 s49, s4, s50\n"
"s_lshl_b32 s49, s49, 1\n"
"s_add_u32 s49, s49, s51\n"
"s_lshl_b32 s50, s49, 1\n"
"v_mov_b32 v22, s50\n"
"s_lshl_b32 s49, s49, 12\n"
"s_lshl_b32 s63, s61, 11\n"
"s_add_u32 s100, s49, s63\n"
"s_add_u32 s3, s100, 8388608\n"
"s_add_u32 s80, s100, 12582912\n"
"v_add_u32 v39, s63, v1\n"
"s_mul_i32 s49, s51, 4224\n"
"s_add_u32 s49, s49, 131072\n"
"s_add_u32 s101, s49, s63\n"
"s_add_u32 s101, s101, 128\n"
"s_xor_b32 s50, s51, 1\n"
"s_mul_i32 s50, s50, 139520\n"
"v_add_u32 v39, s50, v39\n"
"v_and_b32 v222, 0x70, v1\n"
"v_add_u32 v222, s49, v222\n"
"s_mul_i32 s62, s61, 9024\n"
"s_add_u32 s62, s62, s50\n"
"s_add_u32 s62, s62, 4096\n"
"v_add_u32 v40, s62, v1\n"
"v_cmp_lt_u32 vcc, 23, v0\n"
"v_add_u32 v224, 4128, v40\n"
"s_lshr_b32 s62, s63, 3\n"
"v_cndmask_b32 v40, v40, v224, vcc\n"
"v_cmp_lt_u32 vcc, 47, v0\n"
"s_add_u32 s49, s49, s62\n"
"s_add_u32 s49, s49, 8\n"
"v_cndmask_b32 v40, v40, v222, vcc\n"
"v_lshlrev_b32 v222, 3, v225\n"
"v_lshl_add_u32 v222, v229, 4, v222\n"
"v_add_u32 v13, s49, v222\n"
"v_sub_u32 v222, 3, v231\n"
"v_mul_u32_u24 v222, 4512, v222\n"
"v_lshlrev_b32 v224, 7, v230\n"
"v_sub_u32 v222, v222, v224\n"
"v_lshl_add_u32 v222, v229, 4, v222\n"
"s_add_u32 s62, s62, s50\n"
"s_add_u32 s62, s62, 384\n"
"v_add_u32 v12, s62, v222\n"
"s_add_u32 s50, s50, 2552\n"
"s_sub_u32 s50, s50, s63\n"
"v_sub_u32 v14, s50, v44\n"
"s_add_u32 s0, s59, 18432\n"
"s_cmp_eq_u32 s5, 3\n"
"s_cselect_b32 s0, 157696, s0\n"
"v_add_u32 v10, s0, v10\n"
"v_xor_b32 v11, 64, v10\n"
"v_add_u32 v19, s0, v1\n"
"s_xor_b32 s49, s59, 4096\n"
"s_add_u32 s49, s49, 18432\n"
"s_cmp_eq_u32 s5, 2\n"
"s_cselect_b32 s49, 157696, s49\n"
"v_add_u32 v20, s49, v1\n"
"s_lshr_b32 s70, s6, 1\n"
"s_lshl_b32 s70, s70, 3\n"
"s_and_b32 s49, s7, 7\n"
"s_add_u32 s70, s70, s49\n"
"s_and_b32 s77, s6, 1\n"
"s_mul_i32 s77, s77, 5\n"
"s_lshr_b32 s49, s7, 3\n"
"s_add_u32 s77, s77, s49\n"
"s_lshl_b32 s50, s6, 2\n"
"s_and_b32 s63, s7, 3\n"
"s_add_u32 s50, s50, s63\n"
"s_lshr_b32 s63, s7, 2\n"
"s_sub_u32 s63, 10, s63\n"
"s_cmp_eq_u32 s49, 3\n"
"s_cselect_b32 s70, s50, s70\n"
"s_cmp_eq_u32 s49, 3\n"
"s_cselect_b32 s77, s63, s77\n"
"s_lshl_b32 s1, s70, 13\n"
"s_mul_i32 s49, s5, 896\n"
"s_add_u32 s1, s1, s49\n"
"s_add_u32 s1, s1, 17039360\n"
"s_add_u32 s2, s1, 4096\n"
"s_lshl_b32 s49, s70, 1\n"
"s_add_u32 s49, s49, s61\n"
"s_lshl_b32 s50, s77, 1\n"
"s_add_u32 s50, s50, s51\n"
"s_lshl_b32 s81, s49, 15\n"
"s_lshl_b32 s63, s50, 6\n"
"v_lshl_add_u32 v36, v234, 1, s63\n"
"s_add_u32 s81, s81, s63\n"
"s_lshl_b32 s63, s50, 11\n"
"v_add_u32 v45, s63, v15\n"
"s_lshr_b32 s63, s70, 4\n"
"s_lshl_b32 s63, s63, 15\n"
"v_add_u32 v45, s63, v45\n"
"s_lshl_b32 s82, s50, 17\n"
"s_lshl_b32 s63, s49, 6\n"
"s_add_u32 s82, s82, s63\n"
"s_add_u32 s82, s82, 12582912\n"
"s_lshl_b32 s63, s49, 9\n"
"v_add_u32 v46, s63, v37\n"
"s_mul_i32 s49, s5, 458752\n"
"s_lshl_b32 s50, s77, 7\n"
"s_add_u32 s49, s49, s50\n"
"s_mul_i32 s50, s70, 229376\n"
"s_mul_i32 s51, s5, 57344\n"
"s_add_u32 s50, s50, s51\n"
"s_add_u32 s51, s70, s77\n"
"s_and_b32 s51, s51, 3\n"
"s_mov_b32 s62, 0x06534210\n"
"s_cmp_eq_u32 s51, 1\n"
"s_cselect_b32 s62, 0x06453201, s62\n"
"s_cmp_eq_u32 s51, 2\n"
"s_cselect_b32 s62, 0x05643120, s62\n"
"s_cmp_eq_u32 s51, 3\n"
"s_cselect_b32 s62, 0x06542310, s62\n"
"s_min_u32 s76, s6, 6\n"
"s_cmp_eq_u32 s6, 7\n"
"s_cselect_b32 s76, 3, s76\n"
"s_bfe_u32 s51, s62, 0x30000\n"
"s_add_u32 s51, s51, s76\n"
"s_sub_u32 s63, s51, 7\n"
"s_min_u32 s51, s51, s63\n"
"s_lshl_b32 s63, s51, 16\n"
"s_add_u32 s64, s49, s63\n"
"s_lshl_b32 s63, s51, 13\n"
"s_add_u32 s71, s50, s63\n"
"s_bfe_u32 s51, s62, 0x30004\n"
"s_add_u32 s51, s51, s76\n"
"s_sub_u32 s63, s51, 7\n"
"s_min_u32 s51, s51, s63\n"
"s_lshl_b32 s63, s51, 16\n"
"s_add_u32 s65, s49, s63\n"
"s_lshl_b32 s63, s51, 13\n"
"s_add_u32 s72, s50, s63\n"
"s_bfe_u32 s51, s62, 0x30008\n"
"s_add_u32 s51, s51, s76\n"
"s_sub_u32 s63, s51, 7\n"
"s_min_u32 s51, s51, s63\n"
"s_lshl_b32 s63, s51, 16\n"
"s_add_u32 s66, s49, s63\n"
"s_lshl_b32 s63, s51, 13\n"
"s_add_u32 s73, s50, s63\n"
"s_bfe_u32 s51, s62, 0x3000c\n"
"s_add_u32 s51, s51, s76\n"
"s_sub_u32 s63, s51, 7\n"
"s_min_u32 s51, s51, s63\n"
"s_lshl_b32 s63, s51, 16\n"
"s_add_u32 s67, s49, s63\n"
"s_lshl_b32 s63, s51, 13\n"
"s_add_u32 s74, s50, s63\n"
"s_bfe_u32 s51, s62, 0x30010\n"
"s_add_u32 s51, s51, s76\n"
"s_sub_u32 s63, s51, 7\n"
"s_min_u32 s51, s51, s63\n"
"s_lshl_b32 s63, s51, 16\n"
"s_add_u32 s68, s49, s63\n"
"s_lshl_b32 s63, s51, 13\n"
"s_add_u32 s75, s50, s63\n"
"s_bfe_u32 s51, s62, 0x30014\n"
"s_add_u32 s51, s51, s76\n"
"s_mov_b32 s77, s76\n"
"s_sub_u32 s63, s51, 7\n"
"s_min_u32 s51, s51, s63\n"
"s_lshl_b32 s63, s51, 16\n"
"s_add_u32 s69, s49, s63\n"
"s_lshl_b32 s63, s51, 13\n"
"s_add_u32 s76, s50, s63\n"
"s_bfe_u32 s51, s62, 0x30018\n"
"s_add_u32 s51, s51, s77\n"
"s_sub_u32 s63, s51, 7\n"
"s_min_u32 s51, s51, s63\n"
"s_lshl_b32 s63, s51, 16\n"
"s_add_u32 s70, s49, s63\n"
"s_lshl_b32 s63, s51, 13\n"
"s_add_u32 s77, s50, s63\n"
"s_lshl_b32 s49, s56, 1\n"
"s_add_u32 s49, s49, s61\n"
"s_lshl_b32 s50, s57, 1\n"
"s_lshr_b32 s51, s5, 1\n"
"s_add_u32 s50, s50, s51\n"
"s_mul_i32 s16, s49, 229376\n"
"s_lshl_b32 s63, s50, 13\n"
"s_add_u32 s16, s16, s63\n"
"s_add_u32 s17, s16, 4096\n"
"s_lshl_b32 s18, s49, 13\n"
"s_lshl_b32 s63, s50, 7\n"
"v_lshl_add_u32 v43, v234, 1, s63\n"
"s_add_u32 s18, s18, s63\n"
"s_add_u32 s18, s18, 17039360\n"
"s_add_u32 s19, s18, 4096\n"
"s_lshl_b32 s22, s49, 10\n"
"s_add_u32 s22, s22, 16777216\n"
"s_lshl_b32 s23, s50, 9\n"
"s_add_u32 s23, s23, 16842752\n"
"v_mov_b32 v89, 0x3f803f80\n"
"s_waitcnt vmcnt(11)\n"
"v_readfirstlane_b32 s48, v66\n"
"v_dot2_f32_bf16 v216, v76, v89, 0\n"
"buffer_load_dwordx4 v241, s[12:15], s60 offen offset:1024 lds\n"
"v_dot2_f32_bf16 v218, v76, v76, 0\n"
"v_dot2_f32_bf16 v216, v77, v89, v216\n"
"v_dot2_f32_bf16 v218, v77, v77, v218\n"
"v_dot2_f32_bf16 v216, v78, v89, v216\n"
"v_dot2_f32_bf16 v218, v78, v78, v218\n"
"v_dot2_f32_bf16 v216, v79, v89, v216\n"
"v_dot2_f32_bf16 v218, v79, v79, v218\n"
"v_dot2_f32_bf16 v216, v80, v89, v216\n"
"v_dot2_f32_bf16 v218, v80, v80, v218\n"
"v_dot2_f32_bf16 v216, v81, v89, v216\n"
"v_dot2_f32_bf16 v218, v81, v81, v218\n"
"v_dot2_f32_bf16 v216, v82, v89, v216\n"
"buffer_load_dwordx4 v242, s[12:15], s60 offen offset:2048 lds\n"
"v_dot2_f32_bf16 v218, v82, v82, v218\n"
"v_dot2_f32_bf16 v216, v83, v89, v216\n"
"v_dot2_f32_bf16 v218, v83, v83, v218\n"
"s_mul_i32 s48, s48, 1664525\n"
"s_add_u32 s48, s48, 1013904223\n"
"v_add_f32_dpp v216, v216, v216 row_shr:1 row_mask:0xf bank_mask:0xf bound_ctrl:1\n"
"v_add_f32_dpp v218, v218, v218 row_shr:1 row_mask:0xf bank_mask:0xf bound_ctrl:1\n"
"v_mov_b32 v18, s48\n"
"v_add_f32_dpp v216, v216, v216 row_shr:2 row_mask:0xf bank_mask:0xf bound_ctrl:1\n"
"v_add_f32_dpp v218, v218, v218 row_shr:2 row_mask:0xf bank_mask:0xf bound_ctrl:1\n"
"v_mov_b32 v217, s48\n"
"v_add_f32_dpp v216, v216, v216 row_shr:4 row_mask:0xf bank_mask:0xf bound_ctrl:1\n"
"buffer_load_dwordx4 v243, s[12:15], s60 offen offset:3072 lds\n"
"v_add_f32_dpp v218, v218, v218 row_shr:4 row_mask:0xf bank_mask:0xf bound_ctrl:1\n"
"v_mov_b32 v219, s48\n"
"v_add_f32_dpp v216, v216, v216 row_shr:8 row_mask:0xf bank_mask:0xf bound_ctrl:1\n"
"v_add_f32_dpp v218, v218, v218 row_shr:8 row_mask:0xf bank_mask:0xf bound_ctrl:1\n"
"v_lshlrev_b32 v220, 4, v232\n"
"v_add_f32_dpp v216, v216, v216 row_bcast:15 row_mask:0xa bank_mask:0xf\n"
"v_add_f32_dpp v218, v218, v218 row_bcast:15 row_mask:0xa bank_mask:0xf\n"
"v_mul_f32 v216, 0x3b000000, v216\n"
"v_mul_f32 v218, 0x3b000000, v218\n"
"v_fma_f32 v218, -v216, v216, v218\n"
"v_max_f32 v218, 0, v218\n"
"s_branch LALJ\n"
".fill 397, 4, 3212836864\n"
"LALJ:\n"
"s_bfe_u32 s60, s55, 0x30004\n"
"s_add_u32 m0, s59, 32768\n"
"s_lshl_b32 s60, s60, 7\n"
"buffer_load_dwordx4 v236, s[8:11], s60 offen offset:0 lds\n"
"s_waitcnt vmcnt(11)\n"
"ds_read_b64_tr_b16 v[144:145], v209 offset:512\n"
"ds_read_b64_tr_b16 v[146:147], v209 offset:1024\n"
"ds_read_b64_tr_b16 v[148:149], v209 offset:1536\n"
"ds_read_b64_tr_b16 v[150:151], v209 offset:2048\n"
"ds_read_b64_tr_b16 v[152:153], v209 offset:2560\n"
"v_mov_b32 v207, 0xffff\n"
"v_alignbit_b32 v204, v162, v162, 16\n"
"v_alignbit_b32 v205, v163, v163, 16\n"
"v_and_b32 v206, 0xffff, v162\n"
"v_bfi_b32 v207, v207, v163, v162\n"
"v_and_b32 v208, 0xffff0000, v163\n"
"buffer_load_dwordx4 v237, s[8:11], s60 offen offset:1024 lds\n"
"s_waitcnt lgkmcnt(0)\n"
"v_dot2_f32_bf16 v172, v144, v208, 0\n"
"v_dot2_f32_bf16 v173, v145, v205, 0\n"
"v_dot2_f32_bf16 v174, v145, v208, 0\n"
"v_dot2_f32_bf16 v175, v146, v205, 0\n"
"v_dot2_f32_bf16 v176, v146, v208, 0\n"
"v_dot2_f32_bf16 v177, v147, v205, 0\n"
"v_dot2_f32_bf16 v178, v147, v208, 0\n"
"v_dot2_f32_bf16 v179, v148, v205, 0\n"
"v_dot2_f32_bf16 v180, v148, v208, 0\n"
"v_dot2_f32_bf16 v181, v149, v205, 0\n"
"v_dot2_f32_bf16 v182, v149, v208, 0\n"
"buffer_load_dwordx4 v238, s[8:11], s60 offen offset:2048 lds\n"
"v_dot2_f32_bf16 v183, v150, v205, 0\n"
"v_dot2_f32_bf16 v184, v150, v208, 0\n"
"v_dot2_f32_bf16 v185, v151, v205, 0\n"
"v_dot2_f32_bf16 v186, v151, v208, 0\n"
"v_dot2_f32_bf16 v187, v152, v205, 0\n"
"v_dot2_f32_bf16 v172, v145, v207, v172\n"
"v_dot2_f32_bf16 v173, v146, v204, v173\n"
"v_dot2_f32_bf16 v174, v146, v207, v174\n"
"v_dot2_f32_bf16 v175, v147, v204, v175\n"
"s_waitcnt vmcnt(11)\n"
"ds_read_b64_tr_b16 v[154:155], v209 offset:3072\n"
"ds_read_b64_tr_b16 v[156:157], v209 offset:3584\n"
"buffer_load_dwordx4 v239, s[8:11], s60 offen offset:3072 lds\n"
"ds_read_b64_tr_b16 v[158:159], v209 offset:4096\n"
"ds_read_b64_tr_b16 v[160:161], v209 offset:4608\n"
"v_dot2_f32_bf16 v176, v147, v207, v176\n"
"v_dot2_f32_bf16 v177, v148, v204, v177\n"
"v_dot2_f32_bf16 v178, v148, v207, v178\n"
"v_dot2_f32_bf16 v179, v149, v204, v179\n"
"v_dot2_f32_bf16 v180, v149, v207, v180\n"
"v_dot2_f32_bf16 v181, v150, v204, v181\n"
"v_dot2_f32_bf16 v182, v150, v207, v182\n"
"v_dot2_f32_bf16 v183, v151, v204, v183\n"
"v_dot2_f32_bf16 v184, v151, v207, v184\n"
"v_dot2_f32_bf16 v185, v152, v204, v185\n"
"s_add_u32 m0, s59, 49152\n"
"s_mul_i32 s60, s60, 1792\n"
"buffer_load_dwordx4 v240, s[12:15], s60 offen offset:0 lds\n"
"v_dot2_f32_bf16 v186, v152, v207, v186\n"
"v_dot2_f32_bf16 v187, v153, v204, v187\n"
"v_dot2_f32_bf16 v172, v146, v206, v172\n"
"v_dot2_f32_bf16 v174, v147, v206, v174\n"
"v_dot2_f32_bf16 v176, v148, v206, v176\n"
"v_dot2_f32_bf16 v178, v149, v206, v178\n"
"v_dot2_f32_bf16 v180, v150, v206, v180\n"
"v_dot2_f32_bf16 v182, v151, v206, v182\n"
"v_dot2_f32_bf16 v184, v152, v206, v184\n"
"v_dot2_f32_bf16 v186, v153, v206, v186\n"
"s_waitcnt lgkmcnt(0)\n"
"v_lshlrev_b32 v188, 16, v154\n"
"buffer_load_dwordx4 v241, s[12:15], s60 offen offset:1024 lds\n"
"v_and_b32 v189, 0xffff0000, v154\n"
"v_lshlrev_b32 v190, 16, v155\n"
"v_and_b32 v191, 0xffff0000, v155\n"
"v_lshlrev_b32 v192, 16, v156\n"
"v_and_b32 v193, 0xffff0000, v156\n"
"v_lshlrev_b32 v194, 16, v157\n"
"v_and_b32 v195, 0xffff0000, v157\n"
"v_lshlrev_b32 v196, 16, v158\n"
"v_and_b32 v197, 0xffff0000, v158\n"
"v_lshlrev_b32 v198, 16, v159\n"
"v_and_b32 v199, 0xffff0000, v159\n"
"v_lshlrev_b32 v200, 16, v160\n"
"buffer_load_dwordx4 v242, s[12:15], s60 offen offset:2048 lds\n"
"v_and_b32 v201, 0xffff0000, v160\n"
"v_lshlrev_b32 v202, 16, v161\n"
"v_and_b32 v203, 0xffff0000, v161\n"
"v_pk_mul_f32 v[172:173], v[172:173], v[188:189]\n"
"v_pk_mul_f32 v[174:175], v[174:175], v[190:191]\n"
"v_pk_mul_f32 v[176:177], v[176:177], v[192:193]\n"
"v_pk_mul_f32 v[178:179], v[178:179], v[194:195]\n"
"v_pk_mul_f32 v[180:181], v[180:181], v[196:197]\n"
"v_pk_mul_f32 v[182:183], v[182:183], v[198:199]\n"
"v_pk_mul_f32 v[184:185], v[184:185], v[200:201]\n"
"v_pk_mul_f32 v[186:187], v[186:187], v[202:203]\n"
"buffer_load_dwordx4 v243, s[12:15], s60 offen offset:3072 lds\n"
"v_cvt_pk_bf16_f32 v164, v187, v186\n"
"v_cvt_pk_bf16_f32 v165, v185, v184\n"
"v_cvt_pk_bf16_f32 v166, v183, v182\n"
"v_cvt_pk_bf16_f32 v167, v181, v180\n"
"ds_write_b128 v210, v[164:167]\n"
"v_cvt_pk_bf16_f32 v168, v179, v178\n"
"v_cvt_pk_bf16_f32 v169, v177, v176\n"
"v_cvt_pk_bf16_f32 v170, v175, v174\n"
"v_cvt_pk_bf16_f32 v171, v173, v172\n"
"ds_write_b128 v211, v[168:171]\n"
"s_bfe_u32 s60, s55, 0x30008\n"
"s_add_u32 m0, s59, 65536\n"
"s_lshl_b32 s60, s60, 7\n"
"buffer_load_dwordx4 v236, s[8:11], s60 offen offset:0 lds\n"
"buffer_load_dwordx4 v237, s[8:11], s60 offen offset:1024 lds\n"
"buffer_load_dwordx4 v238, s[8:11], s60 offen offset:2048 lds\n"
"buffer_load_dwordx4 v239, s[8:11], s60 offen offset:3072 lds\n"
"s_add_u32 m0, s59, 81920\n"
"s_mul_i32 s60, s60, 1792\n"
"buffer_load_dwordx4 v240, s[12:15], s60 offen offset:0 lds\n"
"buffer_load_dwordx4 v241, s[12:15], s60 offen offset:1024 lds\n"
"s_waitcnt vmcnt(14) lgkmcnt(0)\n"
"s_barrier\n"
"ds_read_b64_tr_b16 v[112:113], v244 offset:0\n"
"ds_read_b64_tr_b16 v[114:115], v244 offset:1024\n"
"ds_read_b128 v[120:123], v248 offset:0\n"
"ds_read_b128 v[124:127], v248 offset:4096\n"
"ds_read_b64_tr_b16 v[116:117], v245 offset:0\n"
"ds_read_b64_tr_b16 v[118:119], v245 offset:1024\n"
"s_waitcnt lgkmcnt(2)\n"
"v_mfma_f32_32x32x16_bf16 v[48:63], v[112:115], v[120:123], 0\n"
"ds_read_b64_tr_b16 v[128:129], v244 offset:4096\n"
"ds_read_b64_tr_b16 v[130:131], v244 offset:5120\n"
"ds_read_b128 v[136:139], v249 offset:0\n"
"v_mfma_f32_32x32x16_bf16 v[64:79], v[112:115], v[124:127], 0\n"
"ds_read_b128 v[140:143], v249 offset:4096\n"
"ds_read_b64_tr_b16 v[132:133], v245 offset:4096\n"
"buffer_load_dwordx4 v242, s[12:15], s60 offen offset:2048 lds\n"
"ds_read_b128 v[144:147], v212\n"
"s_waitcnt lgkmcnt(6)\n"
"v_mfma_f32_32x32x16_bf16 v[96:111], v[116:119], v[124:127], 0\n"
"ds_read_b64_tr_b16 v[134:135], v245 offset:5120\n"
"ds_read_b128 v[148:151], v212 offset:1024\n"
"v_mfma_f32_32x32x16_bf16 v[80:95], v[116:119], v[120:123], 0\n"
"buffer_load_dwordx4 v243, s[12:15], s60 offen offset:3072 lds\n"
"s_nop 10\n"
"s_waitcnt lgkmcnt(4)\n"
"v_mfma_f32_32x32x16_bf16 v[48:63], v[128:131], v[136:139], v[48:63]\n"
"ds_read_b64_tr_b16 v[112:113], v244 offset:8192\n"
"ds_read_b64_tr_b16 v[114:115], v244 offset:9216\n"
"ds_read_b128 v[120:123], v250 offset:0\n"
"v_mfma_f32_32x32x16_bf16 v[64:79], v[128:131], v[140:143], v[64:79]\n"
"ds_read_b128 v[124:127], v250 offset:4096\n"
"ds_read_b64_tr_b16 v[116:117], v245 offset:8192\n"
"s_waitcnt lgkmcnt(5)\n"
"s_add_u32 s40, s54, 16908288\n"
"v_mfma_f32_32x32x16_bf16 v[96:111], v[132:135], v[140:143], v[96:111]\n"
"ds_read_b64_tr_b16 v[118:119], v245 offset:9216\n"
"buffer_store_dwordx4 v[144:147], v214, s[24:27], s79 offen sc1\n"
"s_add_u32 s79, s79, 32768\n"
"v_mfma_f32_32x32x16_bf16 v[80:95], v[132:135], v[136:139], v[80:95]\n"
"buffer_store_dwordx4 v[148:151], v214, s[24:27], s79 offen sc1\n"
"s_bfe_u32 s52, s4, 0x20005\n"
"s_cmp_eq_u32 s52, 3\n"
"s_cselect_b32 s52, 128, 0\n"
"s_xor_b32 s53, s4, s52\n"
"s_nop 5\n"
"s_waitcnt lgkmcnt(2)\n"
"v_mfma_f32_32x32x16_bf16 v[48:63], v[112:115], v[120:123], v[48:63]\n"
"ds_read_b64_tr_b16 v[128:129], v244 offset:12288\n"
"ds_read_b64_tr_b16 v[130:131], v244 offset:13312\n"
"ds_read_b128 v[136:139], v251 offset:0\n"
"v_mfma_f32_32x32x16_bf16 v[64:79], v[112:115], v[124:127], v[64:79]\n"
"ds_read_b128 v[140:143], v251 offset:4096\n"
"ds_read_b64_tr_b16 v[132:133], v245 offset:12288\n"
"s_lshr_b32 s41, s53, 3\n"
"s_lshl_b32 s41, s41, 12\n"
"s_lshl_b32 s52, s5, 10\n"
"s_waitcnt lgkmcnt(5)\n"
"v_mfma_f32_32x32x16_bf16 v[96:111], v[116:119], v[124:127], v[96:111]\n"
"ds_read_b64_tr_b16 v[134:135], v245 offset:13312\n"
"s_add_u32 s41, s41, s52\n"
"s_add_u32 s41, s41, 16908288\n"
"s_lshl_b32 s52, s61, 8\n"
"s_add_u32 s52, s52, s53\n"
"v_mfma_f32_32x32x16_bf16 v[80:95], v[116:119], v[120:123], v[80:95]\n"
"s_mov_b32 exec_lo, 0x80000000\n"
"s_mov_b32 exec_hi, 0x80000000\n"
"buffer_store_dwordx4 v[216:219], v220, s[24:27], s83 offen sc1\n"
"s_mov_b64 exec, -1\n"
"s_mov_b32 m0, s101\n"
"s_nop 5\n"
"s_waitcnt lgkmcnt(2)\n"
"v_mfma_f32_32x32x16_bf16 v[48:63], v[128:131], v[136:139], v[48:63]\n"
"s_waitcnt vmcnt(11) lgkmcnt(0)\n"
"s_barrier\n"
"ds_read_b64_tr_b16 v[112:113], v244 offset:32768\n"
"ds_read_b64_tr_b16 v[114:115], v244 offset:33792\n"
"v_mfma_f32_32x32x16_bf16 v[64:79], v[128:131], v[140:143], v[64:79]\n"
"ds_read_b128 v[120:123], v248 offset:32768\n"
"ds_read_b128 v[124:127], v248 offset:36864\n"
"s_nop 0\n"
"buffer_load_dwordx4 v1, s[32:35], s100 offen lds\n"
"v_mfma_f32_32x32x16_bf16 v[96:111], v[132:135], v[140:143], v[96:111]\n"
"ds_read_b64_tr_b16 v[116:117], v245 offset:32768\n"
"ds_read_b64_tr_b16 v[118:119], v245 offset:33792\n"
"buffer_load_dwordx4 v1, s[32:35], s100 offen offset:1024 lds\n"
"s_lshl_b32 s52, s52, 7\n"
"v_mfma_f32_32x32x16_bf16 v[80:95], v[132:135], v[136:139], v[80:95]\n"
"global_load_ushort v160, v43, s[42:43]\n"
"s_and_b32 s53, s5, 2\n"
"global_load_ushort v161, v43, s[42:43] offset:64\n"
"s_lshl_b32 s53, s53, 1\n"
"global_load_ushort v22, v22, s[44:45]\n"
"s_nop 4\n"
"s_waitcnt lgkmcnt(2)\n"
"v_mfma_f32_32x32x16_bf16 v[48:63], v[112:115], v[120:123], v[48:63]\n"
"ds_read_b64_tr_b16 v[128:129], v244 offset:36864\n"
"ds_read_b64_tr_b16 v[130:131], v244 offset:37888\n"
"ds_read_b128 v[136:139], v249 offset:32768\n"
"v_mfma_f32_32x32x16_bf16 v[64:79], v[112:115], v[124:127], v[64:79]\n"
"ds_read_b128 v[140:143], v249 offset:36864\n"
"ds_read_b64_tr_b16 v[132:133], v245 offset:36864\n"
"global_load_ushort v36, v36, s[46:47]\n"
"s_bfe_u32 s60, s55, 0x3000c\n"
"s_waitcnt lgkmcnt(5)\n"
"v_mfma_f32_32x32x16_bf16 v[96:111], v[116:119], v[124:127], v[96:111]\n"
"ds_read_b64_tr_b16 v[134:135], v245 offset:37888\n"
"s_add_u32 m0, s59, 98304\n"
"s_lshl_b32 s60, s60, 7\n"
"buffer_load_dwordx4 v236, s[8:11], s60 offen offset:0 lds\n"
"v_mfma_f32_32x32x16_bf16 v[80:95], v[116:119], v[120:123], v[80:95]\n"
"buffer_load_dwordx4 v237, s[8:11], s60 offen offset:1024 lds\n"
"s_add_u32 s52, s52, s53\n"
"buffer_load_dwordx4 v238, s[8:11], s60 offen offset:2048 lds\n"
"s_add_u32 s52, s52, 17301504\n"
"s_nop 6\n"
"s_waitcnt lgkmcnt(2)\n"
"v_mfma_f32_32x32x16_bf16 v[48:63], v[128:131], v[136:139], v[48:63]\n"
"ds_read_b64_tr_b16 v[112:113], v244 offset:40960\n"
"ds_read_b64_tr_b16 v[114:115], v244 offset:41984\n"
"ds_read_b128 v[120:123], v250 offset:32768\n"
"v_mfma_f32_32x32x16_bf16 v[64:79], v[128:131], v[140:143], v[64:79]\n"
"ds_read_b128 v[124:127], v250 offset:36864\n"
"ds_read_b64_tr_b16 v[116:117], v245 offset:40960\n"
"buffer_load_dwordx4 v239, s[8:11], s60 offen offset:3072 lds\n"
"s_add_u32 m0, s59, 114688\n"
"s_waitcnt lgkmcnt(5)\n"
"v_mfma_f32_32x32x16_bf16 v[96:111], v[132:135], v[140:143], v[96:111]\n"
"ds_read_b64_tr_b16 v[118:119], v245 offset:41984\n"
"s_mul_i32 s60, s60, 1792\n"
"v_mfma_f32_32x32x16_bf16 v[80:95], v[132:135], v[136:139], v[80:95]\n"
"buffer_load_dwordx4 v240, s[12:15], s60 offen offset:0 lds\n"
"s_nop 10\n"
"s_waitcnt lgkmcnt(2)\n"
"v_mfma_f32_32x32x16_bf16 v[48:63], v[112:115], v[120:123], v[48:63]\n"
"ds_read_b64_tr_b16 v[128:129], v244 offset:45056\n"
"ds_read_b64_tr_b16 v[130:131], v244 offset:46080\n"
"ds_read_b128 v[136:139], v251 offset:32768\n"
"v_mfma_f32_32x32x16_bf16 v[64:79], v[112:115], v[124:127], v[64:79]\n"
"ds_read_b128 v[140:143], v251 offset:36864\n"
"ds_read_b64_tr_b16 v[132:133], v245 offset:45056\n"
"buffer_load_dwordx4 v241, s[12:15], s60 offen offset:1024 lds\n"
"s_waitcnt lgkmcnt(5)\n"
"v_mfma_f32_32x32x16_bf16 v[96:111], v[116:119], v[124:127], v[96:111]\n"
"ds_read_b64_tr_b16 v[134:135], v245 offset:46080\n"
"v_mfma_f32_32x32x16_bf16 v[80:95], v[116:119], v[120:123], v[80:95]\n"
"buffer_load_dwordx4 v242, s[12:15], s60 offen offset:2048 lds\n"
"s_waitcnt vmcnt(14)\n"
"s_mov_b64 exec, 1\n"
"buffer_store_dword v18, off, s[24:27], s40 sc1\n"
"s_mov_b64 exec, -1\n"
"s_nop 4\n"
"s_waitcnt lgkmcnt(2)\n"
"v_mfma_f32_32x32x16_bf16 v[48:63], v[128:131], v[136:139], v[48:63]\n"
"s_waitcnt lgkmcnt(0)\n"
"s_barrier\n"
"ds_read_b64_tr_b16 v[112:113], v246 offset:0\n"
"ds_read_b64_tr_b16 v[114:115], v246 offset:1024\n"
"v_mfma_f32_32x32x16_bf16 v[64:79], v[128:131], v[140:143], v[64:79]\n"
"ds_read_b128 v[120:123], v252 offset:0\n"
"ds_read_b128 v[124:127], v252 offset:4096\n"
"buffer_load_dwordx4 v243, s[12:15], s60 offen offset:3072 lds\n"
"s_bfe_u32 s60, s55, 0x30010\n"
"v_mfma_f32_32x32x16_bf16 v[96:111], v[132:135], v[140:143], v[96:111]\n"
"ds_read_b64_tr_b16 v[116:117], v247 offset:0\n"
"ds_read_b64_tr_b16 v[118:119], v247 offset:1024\n"
"s_add_u32 m0, s59, 0\n"
"s_lshl_b32 s60, s60, 7\n"
"v_mfma_f32_32x32x16_bf16 v[80:95], v[132:135], v[136:139], v[80:95]\n"
"buffer_load_dwordx4 v236, s[8:11], s60 offen offset:0 lds\n"
"s_nop 10\n"
"s_waitcnt lgkmcnt(2)\n"
"v_mfma_f32_32x32x16_bf16 v[48:63], v[112:115], v[120:123], v[48:63]\n"
"ds_read_b64_tr_b16 v[128:129], v246 offset:4096\n"
"ds_read_b64_tr_b16 v[130:131], v246 offset:5120\n"
"ds_read_b128 v[136:139], v253 offset:0\n"
"v_mfma_f32_32x32x16_bf16 v[64:79], v[112:115], v[124:127], v[64:79]\n"
"ds_read_b128 v[140:143], v253 offset:4096\n"
"ds_read_b64_tr_b16 v[132:133], v247 offset:4096\n"
"buffer_load_dwordx4 v237, s[8:11], s60 offen offset:1024 lds\n"
"s_waitcnt lgkmcnt(5)\n"
"v_mfma_f32_32x32x16_bf16 v[96:111], v[116:119], v[124:127], v[96:111]\n"
"ds_read_b64_tr_b16 v[134:135], v247 offset:5120\n"
"v_mfma_f32_32x32x16_bf16 v[80:95], v[116:119], v[120:123], v[80:95]\n"
"buffer_load_dwordx4 v238, s[8:11], s60 offen offset:2048 lds\n"
"s_nop 10\n"
"s_waitcnt lgkmcnt(2)\n"
"v_mfma_f32_32x32x16_bf16 v[48:63], v[128:131], v[136:139], v[48:63]\n"
"ds_read_b64_tr_b16 v[112:113], v246 offset:8192\n"
"ds_read_b64_tr_b16 v[114:115], v246 offset:9216\n"
"ds_read_b128 v[120:123], v254 offset:0\n"
"v_mfma_f32_32x32x16_bf16 v[64:79], v[128:131], v[140:143], v[64:79]\n"
"ds_read_b128 v[124:127], v254 offset:4096\n"
"ds_read_b64_tr_b16 v[116:117], v247 offset:8192\n"
"buffer_load_dwordx4 v239, s[8:11], s60 offen offset:3072 lds\n"
"s_add_u32 m0, s59, 16384\n"
"s_waitcnt lgkmcnt(5)\n"
"v_mfma_f32_32x32x16_bf16 v[96:111], v[132:135], v[140:143], v[96:111]\n"
"ds_read_b64_tr_b16 v[118:119], v247 offset:9216\n"
"s_mul_i32 s60, s60, 1792\n"
"v_mfma_f32_32x32x16_bf16 v[80:95], v[132:135], v[136:139], v[80:95]\n"
"buffer_load_dwordx4 v240, s[12:15], s60 offen offset:0 lds\n"
"s_nop 10\n"
"s_waitcnt lgkmcnt(2)\n"
"v_mfma_f32_32x32x16_bf16 v[48:63], v[112:115], v[120:123], v[48:63]\n"
"ds_read_b64_tr_b16 v[128:129], v246 offset:12288\n"
"ds_read_b64_tr_b16 v[130:131], v246 offset:13312\n"
"ds_read_b128 v[136:139], v255 offset:0\n"
"v_mfma_f32_32x32x16_bf16 v[64:79], v[112:115], v[124:127], v[64:79]\n"
"ds_read_b128 v[140:143], v255 offset:4096\n"
"ds_read_b64_tr_b16 v[132:133], v247 offset:12288\n"
"buffer_load_dwordx4 v241, s[12:15], s60 offen offset:1024 lds\n"
"s_waitcnt vmcnt(17)\n"
"s_waitcnt lgkmcnt(5)\n"
"v_mfma_f32_32x32x16_bf16 v[96:111], v[116:119], v[124:127], v[96:111]\n"
"ds_read_b64_tr_b16 v[134:135], v247 offset:13312\n"
"s_mov_b32 s42, -1\n"
"s_mov_b32 s43, 0\n"
"v_lshl_or_b32 v160, v160, 16, v160\n"
"v_mfma_f32_32x32x16_bf16 v[80:95], v[116:119], v[120:123], v[80:95]\n"
"buffer_load_dwordx4 v242, s[12:15], s60 offen offset:2048 lds\n"
"v_lshl_or_b32 v161, v161, 16, v161\n"
"v_cndmask_b32 v186, 0, v160, s[42:43]\n"
"v_cndmask_b32 v190, 0, v161, s[42:43]\n"
"s_nop 5\n"
"s_waitcnt lgkmcnt(2)\n"
"v_mfma_f32_32x32x16_bf16 v[48:63], v[128:131], v[136:139], v[48:63]\n"
"s_waitcnt vmcnt(7) lgkmcnt(0)\n"
"s_barrier\n"
"ds_read_b64_tr_b16 v[112:113], v246 offset:32768\n"
"ds_read_b64_tr_b16 v[114:115], v246 offset:33792\n"
"v_mfma_f32_32x32x16_bf16 v[64:79], v[128:131], v[140:143], v[64:79]\n"
"ds_read_b128 v[120:123], v252 offset:32768\n"
"ds_read_b128 v[124:127], v252 offset:36864\n"
"buffer_load_dwordx4 v243, s[12:15], s60 offen offset:3072 lds\n"
"s_bfe_u32 s60, s55, 0x30014\n"
"v_mfma_f32_32x32x16_bf16 v[96:111], v[132:135], v[140:143], v[96:111]\n"
"ds_read_b64_tr_b16 v[116:117], v247 offset:32768\n"
"ds_read_b64_tr_b16 v[118:119], v247 offset:33792\n"
"s_add_u32 m0, s59, 32768\n"
"s_lshl_b32 s60, s60, 7\n"
"buffer_load_dwordx4 v[200:203], v37, s[24:27], s22 offen sc1\n"
"v_mfma_f32_32x32x16_bf16 v[80:95], v[132:135], v[136:139], v[80:95]\n"
"buffer_load_dwordx4 v236, s[8:11], s60 offen offset:0 lds\n"
"s_nop 10\n"
"s_waitcnt lgkmcnt(2)\n"
"v_mfma_f32_32x32x16_bf16 v[48:63], v[112:115], v[120:123], v[48:63]\n"
"ds_read_b64_tr_b16 v[128:129], v246 offset:36864\n"
"ds_read_b64_tr_b16 v[130:131], v246 offset:37888\n"
"ds_read_b128 v[136:139], v253 offset:32768\n"
"v_mfma_f32_32x32x16_bf16 v[64:79], v[112:115], v[124:127], v[64:79]\n"
"ds_read_b128 v[140:143], v253 offset:36864\n"
"ds_read_b64_tr_b16 v[132:133], v247 offset:36864\n"
"buffer_load_dwordx4 v237, s[8:11], s60 offen offset:1024 lds\n"
"s_waitcnt lgkmcnt(5)\n"
"v_mfma_f32_32x32x16_bf16 v[96:111], v[116:119], v[124:127], v[96:111]\n"
"ds_read_b64_tr_b16 v[134:135], v247 offset:37888\n"
"buffer_load_dwordx4 v[204:207], v37, s[24:27], s22 offen offset:512 sc1\n"
"v_mfma_f32_32x32x16_bf16 v[80:95], v[116:119], v[120:123], v[80:95]\n"
"buffer_load_dwordx4 v238, s[8:11], s60 offen offset:2048 lds\n"
"s_nop 10\n"
"s_waitcnt lgkmcnt(2)\n"
"v_mfma_f32_32x32x16_bf16 v[48:63], v[128:131], v[136:139], v[48:63]\n"
"ds_read_b64_tr_b16 v[112:113], v246 offset:40960\n"
"ds_read_b64_tr_b16 v[114:115], v246 offset:41984\n"
"ds_read_b128 v[120:123], v254 offset:32768\n"
"v_mfma_f32_32x32x16_bf16 v[64:79], v[128:131], v[140:143], v[64:79]\n"
"ds_read_b128 v[124:127], v254 offset:36864\n"
"ds_read_b64_tr_b16 v[116:117], v247 offset:40960\n"
"buffer_load_dwordx4 v239, s[8:11], s60 offen offset:3072 lds\n"
"s_add_u32 m0, s59, 49152\n"
"s_waitcnt lgkmcnt(5)\n"
"v_mfma_f32_32x32x16_bf16 v[96:111], v[132:135], v[140:143], v[96:111]\n"
"ds_read_b64_tr_b16 v[118:119], v247 offset:41984\n"
"s_mul_i32 s60, s60, 1792\n"
"v_mfma_f32_32x32x16_bf16 v[80:95], v[132:135], v[136:139], v[80:95]\n"
"buffer_load_dwordx4 v240, s[12:15], s60 offen offset:0 lds\n"
"s_nop 10\n"
"s_waitcnt lgkmcnt(2)\n"
"v_mfma_f32_32x32x16_bf16 v[48:63], v[112:115], v[120:123], v[48:63]\n"
"ds_read_b64_tr_b16 v[128:129], v246 offset:45056\n"
"ds_read_b64_tr_b16 v[130:131], v246 offset:46080\n"
"ds_read_b128 v[136:139], v255 offset:32768\n"
"v_mfma_f32_32x32x16_bf16 v[64:79], v[112:115], v[124:127], v[64:79]\n"
"ds_read_b128 v[140:143], v255 offset:36864\n"
"ds_read_b64_tr_b16 v[132:133], v247 offset:45056\n"
"buffer_load_dwordx4 v241, s[12:15], s60 offen offset:1024 lds\n"
"s_waitcnt lgkmcnt(5)\n"
"v_mfma_f32_32x32x16_bf16 v[96:111], v[116:119], v[124:127], v[96:111]\n"
"ds_read_b64_tr_b16 v[134:135], v247 offset:46080\n"
"v_mfma_f32_32x32x16_bf16 v[80:95], v[116:119], v[120:123], v[80:95]\n"
"buffer_load_dwordx4 v242, s[12:15], s60 offen offset:2048 lds\n"
"s_nop 10\n"
"s_waitcnt lgkmcnt(2)\n"
"v_mfma_f32_32x32x16_bf16 v[48:63], v[128:131], v[136:139], v[48:63]\n"
"s_waitcnt vmcnt(9) lgkmcnt(0)\n"
"s_barrier\n"
"ds_read_b64_tr_b16 v[112:113], v244 offset:0\n"
"ds_read_b64_tr_b16 v[114:115], v244 offset:1024\n"
"v_mfma_f32_32x32x16_bf16 v[64:79], v[128:131], v[140:143], v[64:79]\n"
"ds_read_b128 v[120:123], v248 offset:0\n"
"ds_read_b128 v[124:127], v248 offset:4096\n"
"buffer_load_dwordx4 v243, s[12:15], s60 offen offset:3072 lds\n"
"s_bfe_u32 s60, s55, 0x30018\n"
"v_mfma_f32_32x32x16_bf16 v[96:111], v[132:135], v[140:143], v[96:111]\n"
"ds_read_b64_tr_b16 v[116:117], v245 offset:0\n"
"ds_read_b64_tr_b16 v[118:119], v245 offset:1024\n"
"s_add_u32 m0, s59, 65536\n"
"s_lshl_b32 s60, s60, 7\n"
"v_mfma_f32_32x32x16_bf16 v[80:95], v[132:135], v[136:139], v[80:95]\n"
"buffer_load_dwordx4 v236, s[8:11], s60 offen offset:0 lds\n"
"s_nop 10\n"
"s_waitcnt lgkmcnt(2)\n"
"v_mfma_f32_32x32x16_bf16 v[48:63], v[112:115], v[120:123], v[48:63]\n"
"ds_read_b64_tr_b16 v[128:129], v244 offset:4096\n"
"ds_read_b64_tr_b16 v[130:131], v244 offset:5120\n"
"ds_read_b128 v[136:139], v249 offset:0\n"
"v_mfma_f32_32x32x16_bf16 v[64:79], v[112:115], v[124:127], v[64:79]\n"
"ds_read_b128 v[140:143], v249 offset:4096\n"
"ds_read_b64_tr_b16 v[132:133], v245 offset:4096\n"
"buffer_load_dwordx4 v237, s[8:11], s60 offen offset:1024 lds\n"
"s_waitcnt lgkmcnt(5)\n"
"v_mfma_f32_32x32x16_bf16 v[96:111], v[116:119], v[124:127], v[96:111]\n"
"ds_read_b64_tr_b16 v[134:135], v245 offset:5120\n"
"v_mfma_f32_32x32x16_bf16 v[80:95], v[116:119], v[120:123], v[80:95]\n"
"buffer_load_dwordx4 v238, s[8:11], s60 offen offset:2048 lds\n"
"s_nop 7\n"
"s_waitcnt lgkmcnt(2)\n"
"v_mfma_f32_32x32x16_bf16 v[48:63], v[128:131], v[136:139], v[48:63]\n"
"ds_read_b64_tr_b16 v[112:113], v244 offset:8192\n"
"ds_read_b64_tr_b16 v[114:115], v244 offset:9216\n"
"ds_read_b128 v[120:123], v250 offset:0\n"
"v_mfma_f32_32x32x16_bf16 v[64:79], v[128:131], v[140:143], v[64:79]\n"
"ds_read_b128 v[124:127], v250 offset:4096\n"
"ds_read_b64_tr_b16 v[116:117], v245 offset:8192\n"
"buffer_load_dwordx4 v239, s[8:11], s60 offen offset:3072 lds\n"
"s_add_u32 m0, s59, 81920\n"
"s_waitcnt lgkmcnt(5)\n"
"v_mfma_f32_32x32x16_bf16 v[96:111], v[132:135], v[140:143], v[96:111]\n"
"ds_read_b64_tr_b16 v[118:119], v245 offset:9216\n"
"s_mul_i32 s60, s60, 1792\n"
"s_waitcnt vmcnt(10)\n"
"v_max3_u32 v212, v201, v203, v205\n"
"v_mfma_f32_32x32x16_bf16 v[80:95], v[132:135], v[136:139], v[80:95]\n"
"buffer_load_dwordx4 v240, s[12:15], s60 offen offset:0 lds\n"
"v_min3_u32 v213, v201, v203, v205\n"
"v_max3_u32 v212, v212, v207, v18\n"
"v_min3_u32 v213, v213, v207, v18\n"
"s_nop 3\n"
"s_waitcnt lgkmcnt(2)\n"
"v_mfma_f32_32x32x16_bf16 v[48:63], v[112:115], v[120:123], v[48:63]\n"
"ds_read_b64_tr_b16 v[128:129], v244 offset:12288\n"
"ds_read_b64_tr_b16 v[130:131], v244 offset:13312\n"
"ds_read_b128 v[136:139], v251 offset:0\n"
"v_mfma_f32_32x32x16_bf16 v[64:79], v[112:115], v[124:127], v[64:79]\n"
"ds_read_b128 v[140:143], v251 offset:4096\n"
"ds_read_b64_tr_b16 v[132:133], v245 offset:12288\n"
"buffer_load_dwordx4 v241, s[12:15], s60 offen offset:1024 lds\n"
"s_waitcnt lgkmcnt(5)\n"
"v_mfma_f32_32x32x16_bf16 v[96:111], v[116:119], v[124:127], v[96:111]\n"
"ds_read_b64_tr_b16 v[134:135], v245 offset:13312\n"
"v_cmp_ne_u32 vcc, v212, v213\n"
"s_cbranch_vccnz LS1\n"
"LB1:\n"
"v_add_f32 v212, 0x3727c5ac, v202\n"
"v_mfma_f32_32x32x16_bf16 v[80:95], v[116:119], v[120:123], v[80:95]\n"
"buffer_load_dwordx4 v242, s[12:15], s60 offen offset:2048 lds\n"
"v_add_f32 v216, 0x3727c5ac, v206\n"
"v_rsq_f32 v212, v212\n"
"s_nop 3\n"
"s_waitcnt lgkmcnt(2)\n"
"v_mfma_f32_32x32x16_bf16 v[48:63], v[128:131], v[136:139], v[48:63]\n"
"s_waitcnt vmcnt(7) lgkmcnt(0)\n"
"s_barrier\n"
"ds_read_b64_tr_b16 v[112:113], v244 offset:32768\n"
"ds_read_b64_tr_b16 v[114:115], v244 offset:33792\n"
"v_mfma_f32_32x32x16_bf16 v[64:79], v[128:131], v[140:143], v[64:79]\n"
"ds_read_b128 v[120:123], v248 offset:32768\n"
"ds_read_b128 v[124:127], v248 offset:36864\n"
"buffer_load_dwordx4 v243, s[12:15], s60 offen offset:3072 lds\n"
"s_bfe_u32 s60, s55, 0x3001c\n"
"v_mfma_f32_32x32x16_bf16 v[96:111], v[132:135], v[140:143], v[96:111]\n"
"ds_read_b64_tr_b16 v[116:117], v245 offset:32768\n"
"ds_read_b64_tr_b16 v[118:119], v245 offset:33792\n"
"s_add_u32 m0, s59, 98304\n"
"s_lshl_b32 s60, s60, 7\n"
"v_mfma_f32_32x32x16_bf16 v[80:95], v[132:135], v[136:139], v[80:95]\n"
"buffer_load_dwordx4 v236, s[8:11], s60 offen offset:0 lds\n"
"v_rsq_f32 v216, v216\n"
"v_cvt_pk_bf16_f32 v192, -v200, -v200\n"
"v_cvt_pk_bf16_f32 v196, -v204, -v204\n"
"s_nop 2\n"
"s_waitcnt lgkmcnt(2)\n"
"v_mfma_f32_32x32x16_bf16 v[48:63], v[112:115], v[120:123], v[48:63]\n"
"ds_read_b64_tr_b16 v[128:129], v244 offset:36864\n"
"ds_read_b64_tr_b16 v[130:131], v244 offset:37888\n"
"ds_read_b128 v[136:139], v249 offset:32768\n"
"v_mfma_f32_32x32x16_bf16 v[64:79], v[112:115], v[124:127], v[64:79]\n"
"ds_read_b128 v[140:143], v249 offset:36864\n"
"ds_read_b64_tr_b16 v[132:133], v245 offset:36864\n"
"buffer_load_dwordx4 v237, s[8:11], s60 offen offset:1024 lds\n"
"s_waitcnt lgkmcnt(5)\n"
"v_mfma_f32_32x32x16_bf16 v[96:111], v[116:119], v[124:127], v[96:111]\n"
"ds_read_b64_tr_b16 v[134:135], v245 offset:37888\n"
"v_mul_f32 v213, v202, v212\n"
"v_mul_f32 v217, v206, v216\n"
"v_fmaak_f32 v213, v213, v212, 0x3727c5ac\n"
"v_mfma_f32_32x32x16_bf16 v[80:95], v[116:119], v[120:123], v[80:95]\n"
"buffer_load_dwordx4 v238, s[8:11], s60 offen offset:2048 lds\n"
"v_fmaak_f32 v217, v217, v216, 0x3727c5ac\n"
"v_rsq_f32 v213, v213\n"
"s_nop 3\n"
"s_waitcnt lgkmcnt(2)\n"
"v_mfma_f32_32x32x16_bf16 v[48:63], v[128:131], v[136:139], v[48:63]\n"
"ds_read_b64_tr_b16 v[112:113], v244 offset:40960\n"
"ds_read_b64_tr_b16 v[114:115], v244 offset:41984\n"
"ds_read_b128 v[120:123], v250 offset:32768\n"
"v_mfma_f32_32x32x16_bf16 v[64:79], v[128:131], v[140:143], v[64:79]\n"
"ds_read_b128 v[124:127], v250 offset:36864\n"
"ds_read_b64_tr_b16 v[116:117], v245 offset:40960\n"
"buffer_load_dwordx4 v239, s[8:11], s60 offen offset:3072 lds\n"
"s_add_u32 m0, s59, 114688\n"
"s_waitcnt lgkmcnt(5)\n"
"v_mfma_f32_32x32x16_bf16 v[96:111], v[132:135], v[140:143], v[96:111]\n"
"ds_read_b64_tr_b16 v[118:119], v245 offset:41984\n"
"s_mul_i32 s60, s60, 1792\n"
"buffer_load_dwordx2 v[208:209], v42, s[24:27], s23 offen sc1\n"
"v_mfma_f32_32x32x16_bf16 v[80:95], v[132:135], v[136:139], v[80:95]\n"
"buffer_load_dwordx4 v240, s[12:15], s60 offen offset:0 lds\n"
"v_rsq_f32 v217, v217\n"
"buffer_load_dwordx2 v[210:211], v42, s[24:27], s23 offen offset:256 sc1\n"
"s_nop 1\n"
"s_waitcnt lgkmcnt(2)\n"
"v_mfma_f32_32x32x16_bf16 v[48:63], v[112:115], v[120:123], v[48:63]\n"
"ds_read_b64_tr_b16 v[128:129], v244 offset:45056\n"
"ds_read_b64_tr_b16 v[130:131], v244 offset:46080\n"
"ds_read_b128 v[136:139], v251 offset:32768\n"
"v_mfma_f32_32x32x16_bf16 v[64:79], v[112:115], v[124:127], v[64:79]\n"
"ds_read_b128 v[140:143], v251 offset:36864\n"
"ds_read_b64_tr_b16 v[132:133], v245 offset:45056\n"
"buffer_load_dwordx4 v241, s[12:15], s60 offen offset:1024 lds\n"
"s_waitcnt lgkmcnt(5)\n"
"v_mfma_f32_32x32x16_bf16 v[96:111], v[116:119], v[124:127], v[96:111]\n"
"ds_read_b64_tr_b16 v[134:135], v245 offset:46080\n"
"v_mov_b32 v21, v18\n"
"s_mov_b64 exec, 255\n"
"buffer_load_dword v21, v15, s[24:27], s41 offen sc1\n"
"s_mov_b64 exec, -1\n"
"v_mfma_f32_32x32x16_bf16 v[80:95], v[116:119], v[120:123], v[80:95]\n"
"buffer_load_dwordx4 v242, s[12:15], s60 offen offset:2048 lds\n"
"v_mov_b32 v224, 0\n"
"v_mov_b32 v225, 0\n"
"v_mov_b32 v226, 0\n"
"v_mov_b32 v227, 0\n"
"s_nop 2\n"
"s_waitcnt lgkmcnt(2)\n"
"v_mfma_f32_32x32x16_bf16 v[48:63], v[128:131], v[136:139], v[48:63]\n"
"s_waitcnt vmcnt(10) lgkmcnt(0)\n"
"s_barrier\n"
"ds_read_b64_tr_b16 v[112:113], v246 offset:0\n"
"ds_read_b64_tr_b16 v[114:115], v246 offset:1024\n"
"v_mfma_f32_32x32x16_bf16 v[64:79], v[128:131], v[140:143], v[64:79]\n"
"ds_read_b128 v[120:123], v252 offset:0\n"
"ds_read_b128 v[124:127], v252 offset:4096\n"
"buffer_load_dwordx4 v243, s[12:15], s60 offen offset:3072 lds\n"
"s_mov_b32 m0, s0\n"
"v_mfma_f32_32x32x16_bf16 v[96:111], v[132:135], v[140:143], v[96:111]\n"
"ds_read_b64_tr_b16 v[116:117], v247 offset:0\n"
"ds_read_b64_tr_b16 v[118:119], v247 offset:1024\n"
"v_and_b32 v214, 0xffff0000, v192\n"
"v_and_b32 v218, 0xffff0000, v196\n"
"v_mfma_f32_32x32x16_bf16 v[80:95], v[132:135], v[136:139], v[80:95]\n"
"buffer_load_dwordx4 v2, s[28:31], s64 offen offset:0 lds\n"
"ds_write_b128 v40, v[224:227]\n"
"s_nop 1\n"
"s_waitcnt lgkmcnt(3)\n"
"v_mfma_f32_32x32x16_bf16 v[48:63], v[112:115], v[120:123], v[48:63]\n"
"ds_read_b64_tr_b16 v[128:129], v246 offset:4096\n"
"ds_read_b64_tr_b16 v[130:131], v246 offset:5120\n"
"ds_read_b128 v[136:139], v253 offset:0\n"
"v_mfma_f32_32x32x16_bf16 v[64:79], v[112:115], v[124:127], v[64:79]\n"
"ds_read_b128 v[140:143], v253 offset:4096\n"
"ds_read_b64_tr_b16 v[132:133], v247 offset:4096\n"
"buffer_load_dwordx4 v3, s[28:31], s64 offen offset:1024 lds\n"
"s_waitcnt lgkmcnt(6)\n"
"v_mfma_f32_32x32x16_bf16 v[96:111], v[116:119], v[124:127], v[96:111]\n"
"ds_read_b64_tr_b16 v[134:135], v247 offset:5120\n"
"v_mul_f32 v212, v212, v213\n"
"v_mul_f32 v216, v216, v217\n"
"v_mfma_f32_32x32x16_bf16 v[80:95], v[116:119], v[120:123], v[80:95]\n"
"buffer_load_dwordx4 v4, s[28:31], s64 offen offset:2048 lds\n"
"v_rcp_f32 v178, v212\n"
"v_rcp_f32 v182, v216\n"
"s_waitcnt lgkmcnt(2)\n"
"v_mfma_f32_32x32x16_bf16 v[48:63], v[128:131], v[136:139], v[48:63]\n"
"ds_read_b64_tr_b16 v[112:113], v246 offset:8192\n"
"ds_read_b64_tr_b16 v[114:115], v246 offset:9216\n"
"ds_read_b128 v[120:123], v254 offset:0\n"
"v_mfma_f32_32x32x16_bf16 v[64:79], v[128:131], v[140:143], v[64:79]\n"
"ds_read_b128 v[124:127], v254 offset:4096\n"
"ds_read_b64_tr_b16 v[116:117], v247 offset:8192\n"
"buffer_load_dwordx4 v5, s[28:31], s64 offen offset:3072 lds\n"
"s_add_u32 m0, s78, 32768\n"
"s_waitcnt lgkmcnt(5)\n"
"v_mfma_f32_32x32x16_bf16 v[96:111], v[132:135], v[140:143], v[96:111]\n"
"ds_read_b64_tr_b16 v[118:119], v247 offset:9216\n"
"v_add_f32 v214, v200, v214\n"
"v_add_f32 v218, v204, v218\n"
"v_cvt_pk_bf16_f32 v193, -v214, -v214\n"
"v_mfma_f32_32x32x16_bf16 v[80:95], v[132:135], v[136:139], v[80:95]\n"
"buffer_load_dwordx4 v2, s[28:31], s65 offen offset:0 lds\n"
"v_cvt_pk_bf16_f32 v197, -v218, -v218\n"
"v_mul_f32 v177, 0xc0135761, v212\n"
"v_mul_f32 v181, 0xc0135761, v216\n"
"v_and_b32 v195, 0xffff0000, v193\n"
"s_waitcnt lgkmcnt(2)\n"
"v_mfma_f32_32x32x16_bf16 v[48:63], v[112:115], v[120:123], v[48:63]\n"
"ds_read_b64_tr_b16 v[128:129], v246 offset:12288\n"
"ds_read_b64_tr_b16 v[130:131], v246 offset:13312\n"
"ds_read_b128 v[136:139], v255 offset:0\n"
"v_mfma_f32_32x32x16_bf16 v[64:79], v[112:115], v[124:127], v[64:79]\n"
"ds_read_b128 v[140:143], v255 offset:4096\n"
"ds_read_b64_tr_b16 v[132:133], v247 offset:12288\n"
"buffer_load_dwordx4 v3, s[28:31], s65 offen offset:1024 lds\n"
"s_waitcnt lgkmcnt(5)\n"
"v_mfma_f32_32x32x16_bf16 v[96:111], v[116:119], v[124:127], v[96:111]\n"
"ds_read_b64_tr_b16 v[134:135], v247 offset:13312\n"
"v_and_b32 v199, 0xffff0000, v197\n"
"v_add_f32 v214, v214, v195\n"
"v_add_f32 v218, v218, v199\n"
"v_mfma_f32_32x32x16_bf16 v[80:95], v[116:119], v[120:123], v[80:95]\n"
"buffer_load_dwordx4 v4, s[28:31], s65 offen offset:2048 lds\n"
"s_waitcnt vmcnt(11)\n"
"v_max3_u32 v222, v209, v211, v18\n"
"v_min3_u32 v223, v209, v211, v18\n"
"v_cvt_pk_bf16_f32 v195, -v200, -v214\n"
"s_waitcnt lgkmcnt(2)\n"
"v_mfma_f32_32x32x16_bf16 v[48:63], v[128:131], v[136:139], v[48:63]\n"
"s_waitcnt vmcnt(7) lgkmcnt(0)\n"
"s_barrier\n"
"ds_read_b64_tr_b16 v[112:113], v246 offset:32768\n"
"ds_read_b64_tr_b16 v[114:115], v246 offset:33792\n"
"v_mfma_f32_32x32x16_bf16 v[64:79], v[128:131], v[140:143], v[64:79]\n"
"ds_read_b128 v[120:123], v252 offset:32768\n"
"ds_read_b128 v[124:127], v252 offset:36864\n"
"buffer_load_dwordx4 v5, s[28:31], s65 offen offset:3072 lds\n"
"s_add_u32 m0, s78, 36864\n"
"v_mfma_f32_32x32x16_bf16 v[96:111], v[132:135], v[140:143], v[96:111]\n"
"ds_read_b64_tr_b16 v[116:117], v247 offset:32768\n"
"ds_read_b64_tr_b16 v[118:119], v247 offset:33792\n"
"v_cvt_pk_bf16_f32 v199, -v204, -v218\n"
"v_mfma_f32_32x32x16_bf16 v[80:95], v[132:135], v[136:139], v[80:95]\n"
"buffer_load_dwordx4 v2, s[28:31], s66 offen offset:0 lds\n"
"v_cmp_ne_u32 vcc, v18, v21\n"
"s_cbranch_vccnz LS0\n"
"LB0:\n"
"s_barrier\n"
"v_mul_f32 v213, v212, v212\n"
"s_waitcnt lgkmcnt(2)\n"
"v_mfma_f32_32x32x16_bf16 v[48:63], v[112:115], v[120:123], v[48:63]\n"
"ds_read_b64_tr_b16 v[128:129], v246 offset:36864\n"
"ds_read_b64_tr_b16 v[130:131], v246 offset:37888\n"
"ds_read_b128 v[136:139], v253 offset:32768\n"
"v_mfma_f32_32x32x16_bf16 v[64:79], v[112:115], v[124:127], v[64:79]\n"
"ds_read_b128 v[140:143], v253 offset:36864\n"
"ds_read_b64_tr_b16 v[132:133], v247 offset:36864\n"
"buffer_load_dwordx4 v3, s[28:31], s66 offen offset:1024 lds\n"
"s_waitcnt lgkmcnt(5)\n"
"v_mfma_f32_32x32x16_bf16 v[96:111], v[116:119], v[124:127], v[96:111]\n"
"ds_read_b64_tr_b16 v[134:135], v247 offset:37888\n"
"buffer_load_dwordx4 v[160:163], v1, s[24:27], s3 offen offset:0 sc1\n"
"v_mul_f32 v217, v216, v216\n"
"v_mfma_f32_32x32x16_bf16 v[80:95], v[116:119], v[120:123], v[80:95]\n"
"buffer_load_dwordx4 v4, s[28:31], s66 offen offset:2048 lds\n"
"v_cmp_ne_u32 vcc, v222, v223\n"
"s_cbranch_vccnz LS2\n"
"LB2:\n"
"buffer_load_dwordx4 v[168:171], v1, s[24:27], s3 offen offset:1024 sc1\n"
"s_waitcnt lgkmcnt(2)\n"
"v_mfma_f32_32x32x16_bf16 v[48:63], v[128:131], v[136:139], v[48:63]\n"
"ds_read_b64_tr_b16 v[112:113], v246 offset:40960\n"
"ds_read_b64_tr_b16 v[114:115], v246 offset:41984\n"
"ds_read_b128 v[120:123], v254 offset:32768\n"
"v_mfma_f32_32x32x16_bf16 v[64:79], v[128:131], v[140:143], v[64:79]\n"
"ds_read_b128 v[124:127], v254 offset:36864\n"
"ds_read_b64_tr_b16 v[116:117], v247 offset:40960\n"
"buffer_load_dwordx4 v5, s[28:31], s66 offen offset:3072 lds\n"
"s_add_u32 m0, s78, 65536\n"
"s_waitcnt lgkmcnt(5)\n"
"v_mfma_f32_32x32x16_bf16 v[96:111], v[132:135], v[140:143], v[96:111]\n"
"ds_read_b64_tr_b16 v[118:119], v247 offset:41984\n"
"v_cvt_pk_bf16_f32 v222, v208, v208\n"
"v_cvt_pk_bf16_f32 v223, v210, v210\n"
"v_and_b32 v222, 0xffff0000, v222\n"
"v_mfma_f32_32x32x16_bf16 v[80:95], v[132:135], v[136:139], v[80:95]\n"
"buffer_load_dwordx4 v2, s[28:31], s67 offen offset:0 lds\n"
"v_and_b32 v223, 0xffff0000, v223\n"
"v_sub_f32 v187, v208, v222\n"
"v_sub_f32 v191, v210, v223\n"
"v_cvt_pk_bf16_f32 v222, v208, v187\n"
"s_waitcnt lgkmcnt(2)\n"
"v_mfma_f32_32x32x16_bf16 v[48:63], v[112:115], v[120:123], v[48:63]\n"
"ds_read_b64_tr_b16 v[128:129], v246 offset:45056\n"
"ds_read_b64_tr_b16 v[130:131], v246 offset:46080\n"
"ds_read_b128 v[136:139], v255 offset:32768\n"
"v_mfma_f32_32x32x16_bf16 v[64:79], v[112:115], v[124:127], v[64:79]\n"
"ds_read_b128 v[140:143], v255 offset:36864\n"
"ds_read_b64_tr_b16 v[132:133], v247 offset:45056\n"
"buffer_load_dwordx4 v3, s[28:31], s67 offen offset:1024 lds\n"
"s_waitcnt vmcnt(12)\n"
"s_waitcnt lgkmcnt(5)\n"
"v_mfma_f32_32x32x16_bf16 v[96:111], v[116:119], v[124:127], v[96:111]\n"
"ds_read_b64_tr_b16 v[134:135], v247 offset:46080\n"
"v_cvt_pk_bf16_f32 v223, v210, v191\n"
"v_cndmask_b32 v184, 0, v222, s[42:43]\n"
"v_cndmask_b32 v188, 0, v223, s[42:43]\n"
"v_mfma_f32_32x32x16_bf16 v[80:95], v[116:119], v[120:123], v[80:95]\n"
"buffer_load_dwordx4 v4, s[28:31], s67 offen offset:2048 lds\n"
"v_cndmask_b32 v185, 0, v222, s[42:43]\n"
"v_cndmask_b32 v189, 0, v223, s[42:43]\n"
"v_and_b32 v222, 0xffff0000, v222\n"
"v_and_b32 v223, 0xffff0000, v223\n"
"s_waitcnt lgkmcnt(2)\n"
"v_mfma_f32_32x32x16_bf16 v[48:63], v[128:131], v[136:139], v[48:63]\n"
"s_waitcnt lgkmcnt(0)\n"
"s_barrier\n"
"v_sub_f32 v222, v187, v222\n"
"v_sub_f32 v223, v191, v223\n"
"v_cvt_pk_bf16_f32 v222, v222, v208\n"
"v_mfma_f32_32x32x16_bf16 v[64:79], v[128:131], v[140:143], v[64:79]\n"
"buffer_load_dwordx4 v5, s[28:31], s67 offen offset:3072 lds\n"
"s_add_u32 m0, s78, 69632\n"
"v_cvt_pk_bf16_f32 v223, v223, v210\n"
"v_cndmask_b32 v187, 0, v222, s[42:43]\n"
"v_cndmask_b32 v191, 0, v223, s[42:43]\n"
"v_mfma_f32_32x32x16_bf16 v[96:111], v[132:135], v[140:143], v[96:111]\n"
"v_cvt_pk_bf16_f32 v214, v178, v178\n"
"v_cvt_pk_bf16_f32 v218, v182, v182\n"
"v_mul_f32 v213, 0x3d372713, v213\n"
"v_mul_f32 v217, 0x3d372713, v217\n"
"v_and_b32 v214, 0xffff0000, v214\n"
"v_and_b32 v218, 0xffff0000, v218\n"
"v_mfma_f32_32x32x16_bf16 v[80:95], v[132:135], v[136:139], v[80:95]\n"
"buffer_load_dwordx4 v2, s[28:31], s68 offen offset:0 lds\n"
"v_mul_f32 v176, v213, v177\n"
"v_mul_f32 v180, v217, v181\n"
"v_sub_f32 v214, v178, v214\n"
"v_sub_f32 v218, v182, v218\n"
"v_cvt_pk_bf16_f32 v194, v178, v214\n"
"v_cvt_pk_bf16_f32 v198, v182, v218\n"
"s_nop 0\n"
"v_mfma_f32_32x32x16_bf16 v[48:63], v[184:187], v[192:195], v[48:63]\n"
"ds_read_b64_tr_b16 a[0:1], v10 offset:0\n"
"ds_read_b64_tr_b16 a[4:5], v11 offset:0\n"
"ds_read_b64_tr_b16 a[2:3], v10 offset:1024\n"
"ds_read_b64_tr_b16 a[6:7], v11 offset:1024\n"
"v_mfma_f32_32x32x16_bf16 v[64:79], v[184:187], v[196:199], v[64:79]\n"
"buffer_load_dwordx4 v3, s[28:31], s68 offen offset:1024 lds\n"
"ds_read_b64_tr_b16 a[8:9], v10 offset:2048\n"
"ds_read_b64_tr_b16 a[12:13], v11 offset:2048\n"
"ds_read_b64_tr_b16 a[10:11], v10 offset:3072\n"
"v_mfma_f32_32x32x16_bf16 v[80:95], v[188:191], v[192:195], v[80:95]\n"
"ds_read_b64_tr_b16 a[14:15], v11 offset:3072\n"
"s_waitcnt vmcnt(12)\n"
"ds_read_b64_tr_b16 a[16:17], v6 offset:32768\n"
"ds_read_b64_tr_b16 a[20:21], v7 offset:32768\n"
"v_mfma_f32_32x32x16_bf16 v[96:111], v[188:191], v[196:199], v[96:111]\n"
"buffer_load_dwordx4 v4, s[28:31], s68 offen offset:2048 lds\n"
"ds_read_b64_tr_b16 a[18:19], v6 offset:33792\n"
"ds_read_b64_tr_b16 a[22:23], v7 offset:33792\n"
"ds_read_b64_tr_b16 a[24:25], v6 offset:34816\n"
"v_pk_mul_f32 v[112:113], v[48:49], v[48:49]\n"
"v_pk_mul_f32 v[114:115], v[50:51], v[50:51]\n"
"buffer_load_dwordx4 v5, s[28:31], s68 offen offset:3072 lds\n"
"v_pk_mul_f32 v[116:117], v[52:53], v[52:53]\n"
"v_pk_mul_f32 v[118:119], v[54:55], v[54:55]\n"
"v_pk_fma_f32 v[112:113], v[112:113], v[176:177], v[176:177] op_sel:[0,0,1] op_sel_hi:[1,0,1]\n"
"v_pk_fma_f32 v[114:115], v[114:115], v[176:177], v[176:177] op_sel:[0,0,1] op_sel_hi:[1,0,1]\n"
"v_pk_fma_f32 v[116:117], v[116:117], v[176:177], v[176:177] op_sel:[0,0,1] op_sel_hi:[1,0,1]\n"
"v_pk_fma_f32 v[118:119], v[118:119], v[176:177], v[176:177] op_sel:[0,0,1] op_sel_hi:[1,0,1]\n"
"v_pk_mul_f32 v[112:113], v[112:113], v[48:49]\n"
"v_pk_mul_f32 v[114:115], v[114:115], v[50:51]\n"
"v_pk_mul_f32 v[116:117], v[116:117], v[52:53]\n"
"v_pk_mul_f32 v[118:119], v[118:119], v[54:55]\n"
"v_exp_f32 v128, v112\n"
"v_exp_f32 v129, v113\n"
"v_exp_f32 v130, v114\n"
"v_exp_f32 v131, v115\n"
"v_pk_fma_f32 v[128:129], v[128:129], v[178:179], v[178:179] op_sel:[0,0,0] op_sel_hi:[1,0,0]\n"
"s_add_u32 m0, s78, 98304\n"
"s_nop 0\n"
"buffer_load_dwordx4 v2, s[28:31], s69 offen offset:0 lds\n"
"v_exp_f32 v132, v116\n"
"v_exp_f32 v133, v117\n"
"v_pk_fma_f32 v[130:131], v[130:131], v[178:179], v[178:179] op_sel:[0,0,0] op_sel_hi:[1,0,0]\n"
"v_exp_f32 v134, v118\n"
"v_exp_f32 v135, v119\n"
"v_pk_fma_f32 v[132:133], v[132:133], v[178:179], v[178:179] op_sel:[0,0,0] op_sel_hi:[1,0,0]\n"
"v_pk_mul_f32 v[120:121], v[56:57], v[56:57]\n"
"v_rcp_f32 v128, v128\n"
"v_rcp_f32 v129, v129\n"
"v_pk_fma_f32 v[134:135], v[134:135], v[178:179], v[178:179] op_sel:[0,0,0] op_sel_hi:[1,0,0]\n"
"v_pk_mul_f32 v[122:123], v[58:59], v[58:59]\n"
"v_pk_mul_f32 v[124:125], v[60:61], v[60:61]\n"
"v_rcp_f32 v130, v130\n"
"v_rcp_f32 v131, v131\n"
"v_pk_mul_f32 v[126:127], v[62:63], v[62:63]\n"
"buffer_load_dwordx4 v3, s[28:31], s69 offen offset:1024 lds\n"
"v_pk_fma_f32 v[120:121], v[120:121], v[176:177], v[176:177] op_sel:[0,0,1] op_sel_hi:[1,0,1]\n"
"v_pk_fma_f32 v[122:123], v[122:123], v[176:177], v[176:177] op_sel:[0,0,1] op_sel_hi:[1,0,1]\n"
"v_rcp_f32 v132, v132\n"
"v_rcp_f32 v133, v133\n"
"v_pk_fma_f32 v[124:125], v[124:125], v[176:177], v[176:177] op_sel:[0,0,1] op_sel_hi:[1,0,1]\n"
"v_pk_fma_f32 v[126:127], v[126:127], v[176:177], v[176:177] op_sel:[0,0,1] op_sel_hi:[1,0,1]\n"
"v_pk_mul_f32 v[120:121], v[120:121], v[56:57]\n"
"v_rcp_f32 v134, v134\n"
"v_rcp_f32 v135, v135\n"
"v_pk_mul_f32 v[122:123], v[122:123], v[58:59]\n"
"v_pk_mul_f32 v[124:125], v[124:125], v[60:61]\n"
"v_pk_mul_f32 v[126:127], v[126:127], v[62:63]\n"
"v_exp_f32 v136, v120\n"
"v_exp_f32 v137, v121\n"
"v_pk_mul_f32 v[128:129], v[128:129], v[48:49]\n"
"buffer_load_dwordx4 v4, s[28:31], s69 offen offset:2048 lds\n"
"v_pk_mul_f32 v[130:131], v[130:131], v[50:51]\n"
"v_pk_mul_f32 v[132:133], v[132:133], v[52:53]\n"
"v_exp_f32 v138, v122\n"
"v_exp_f32 v139, v123\n"
"v_pk_mul_f32 v[134:135], v[134:135], v[54:55]\n"
"v_pk_fma_f32 v[136:137], v[136:137], v[178:179], v[178:179] op_sel:[0,0,0] op_sel_hi:[1,0,0]\n"
"v_cvt_pk_bf16_f32 v144, v128, v129\n"
"v_exp_f32 v140, v124\n"
"v_exp_f32 v141, v125\n"
"v_cvt_pk_bf16_f32 v145, v130, v131\n"
"v_pk_fma_f32 v[138:139], v[138:139], v[178:179], v[178:179] op_sel:[0,0,0] op_sel_hi:[1,0,0]\n"
"v_cvt_pk_bf16_f32 v146, v132, v133\n"
"v_exp_f32 v142, v126\n"
"v_exp_f32 v143, v127\n"
"v_cvt_pk_bf16_f32 v147, v134, v135\n"
"buffer_load_dwordx4 v5, s[28:31], s69 offen offset:3072 lds\n"
"buffer_store_dwordx4 v[144:147], v1, s[24:27], s16 offen offset:0 sc1\n"
"v_pk_fma_f32 v[140:141], v[140:141], v[178:179], v[178:179] op_sel:[0,0,0] op_sel_hi:[1,0,0]\n"
"v_pk_mul_f32 v[112:113], v[64:65], v[64:65]\n"
"v_rcp_f32 v136, v136\n"
"v_rcp_f32 v137, v137\n"
"v_pk_fma_f32 v[142:143], v[142:143], v[178:179], v[178:179] op_sel:[0,0,0] op_sel_hi:[1,0,0]\n"
"v_pk_mul_f32 v[114:115], v[66:67], v[66:67]\n"
"v_pk_mul_f32 v[116:117], v[68:69], v[68:69]\n"
"v_rcp_f32 v138, v138\n"
"v_rcp_f32 v139, v139\n"
"v_pk_mul_f32 v[118:119], v[70:71], v[70:71]\n"
"v_pk_fma_f32 v[112:113], v[112:113], v[180:181], v[180:181] op_sel:[0,0,1] op_sel_hi:[1,0,1]\n"
"v_pk_fma_f32 v[114:115], v[114:115], v[180:181], v[180:181] op_sel:[0,0,1] op_sel_hi:[1,0,1]\n"
"v_rcp_f32 v140, v140\n"
"v_rcp_f32 v141, v141\n"
"s_add_u32 m0, s78, 102400\n"
"s_nop 0\n"
"buffer_load_dwordx4 v2, s[28:31], s70 offen offset:0 lds\n"
"v_pk_fma_f32 v[116:117], v[116:117], v[180:181], v[180:181] op_sel:[0,0,1] op_sel_hi:[1,0,1]\n"
"v_pk_fma_f32 v[118:119], v[118:119], v[180:181], v[180:181] op_sel:[0,0,1] op_sel_hi:[1,0,1]\n"
"v_pk_mul_f32 v[112:113], v[112:113], v[64:65]\n"
"v_rcp_f32 v142, v142\n"
"v_rcp_f32 v143, v143\n"
"v_pk_mul_f32 v[114:115], v[114:115], v[66:67]\n"
"v_pk_mul_f32 v[116:117], v[116:117], v[68:69]\n"
"v_pk_mul_f32 v[118:119], v[118:119], v[70:71]\n"
"v_exp_f32 v128, v112\n"
"v_exp_f32 v129, v113\n"
"v_pk_mul_f32 v[136:137], v[136:137], v[56:57]\n"
"v_pk_mul_f32 v[138:139], v[138:139], v[58:59]\n"
"v_pk_mul_f32 v[140:141], v[140:141], v[60:61]\n"
"v_exp_f32 v130, v114\n"
"v_exp_f32 v131, v115\n"
"buffer_load_dwordx4 v3, s[28:31], s70 offen offset:1024 lds\n"
"v_pk_mul_f32 v[142:143], v[142:143], v[62:63]\n"
"v_pk_fma_f32 v[128:129], v[128:129], v[182:183], v[182:183] op_sel:[0,0,0] op_sel_hi:[1,0,0]\n"
"v_cvt_pk_bf16_f32 v148, v136, v137\n"
"v_exp_f32 v132, v116\n"
"v_exp_f32 v133, v117\n"
"v_cvt_pk_bf16_f32 v149, v138, v139\n"
"v_pk_fma_f32 v[130:131], v[130:131], v[182:183], v[182:183] op_sel:[0,0,0] op_sel_hi:[1,0,0]\n"
"v_cvt_pk_bf16_f32 v150, v140, v141\n"
"v_exp_f32 v134, v118\n"
"v_exp_f32 v135, v119\n"
"v_cvt_pk_bf16_f32 v151, v142, v143\n"
"buffer_store_dwordx4 v[148:151], v1, s[24:27], s16 offen offset:2048 sc1\n"
"v_pk_fma_f32 v[132:133], v[132:133], v[182:183], v[182:183] op_sel:[0,0,0] op_sel_hi:[1,0,0]\n"
"v_pk_mul_f32 v[120:121], v[72:73], v[72:73]\n"
"v_rcp_f32 v128, v128\n"
"buffer_load_dwordx4 v4, s[28:31], s70 offen offset:2048 lds\n"
"v_rcp_f32 v129, v129\n"
"v_pk_fma_f32 v[134:135], v[134:135], v[182:183], v[182:183] op_sel:[0,0,0] op_sel_hi:[1,0,0]\n"
"v_pk_mul_f32 v[122:123], v[74:75], v[74:75]\n"
"v_pk_mul_f32 v[124:125], v[76:77], v[76:77]\n"
"v_rcp_f32 v130, v130\n"
"v_rcp_f32 v131, v131\n"
"v_pk_mul_f32 v[126:127], v[78:79], v[78:79]\n"
"v_pk_fma_f32 v[120:121], v[120:121], v[180:181], v[180:181] op_sel:[0,0,1] op_sel_hi:[1,0,1]\n"
"v_pk_fma_f32 v[122:123], v[122:123], v[180:181], v[180:181] op_sel:[0,0,1] op_sel_hi:[1,0,1]\n"
"v_rcp_f32 v132, v132\n"
"v_rcp_f32 v133, v133\n"
"v_pk_fma_f32 v[124:125], v[124:125], v[180:181], v[180:181] op_sel:[0,0,1] op_sel_hi:[1,0,1]\n"
"v_pk_fma_f32 v[126:127], v[126:127], v[180:181], v[180:181] op_sel:[0,0,1] op_sel_hi:[1,0,1]\n"
"v_pk_mul_f32 v[120:121], v[120:121], v[72:73]\n"
"v_rcp_f32 v134, v134\n"
"buffer_load_dwordx4 v5, s[28:31], s70 offen offset:3072 lds\n"
"v_rcp_f32 v135, v135\n"
"v_pk_mul_f32 v[122:123], v[122:123], v[74:75]\n"
"v_pk_mul_f32 v[124:125], v[124:125], v[76:77]\n"
"v_pk_mul_f32 v[126:127], v[126:127], v[78:79]\n"
"v_exp_f32 v136, v120\n"
"v_exp_f32 v137, v121\n"
"v_pk_mul_f32 v[128:129], v[128:129], v[64:65]\n"
"v_pk_mul_f32 v[130:131], v[130:131], v[66:67]\n"
"v_pk_mul_f32 v[132:133], v[132:133], v[68:69]\n"
"v_exp_f32 v138, v122\n"
"v_exp_f32 v139, v123\n"
"v_pk_mul_f32 v[134:135], v[134:135], v[70:71]\n"
"v_pk_fma_f32 v[136:137], v[136:137], v[182:183], v[182:183] op_sel:[0,0,0] op_sel_hi:[1,0,0]\n"
"v_cvt_pk_bf16_f32 v152, v128, v129\n"
"v_exp_f32 v140, v124\n"
"s_waitcnt lgkmcnt(5)\n"
"s_mov_b32 m0, s0\n"
"s_add_u32 s64, s64, 32768\n"
"buffer_load_dwordx4 v2, s[28:31], s64 offen offset:0 lds\n"
"v_exp_f32 v141, v125\n"
"v_cvt_pk_bf16_f32 v153, v130, v131\n"
"v_pk_fma_f32 v[138:139], v[138:139], v[182:183], v[182:183] op_sel:[0,0,0] op_sel_hi:[1,0,0]\n"
"v_cvt_pk_bf16_f32 v154, v132, v133\n"
"v_exp_f32 v142, v126\n"
"v_exp_f32 v143, v127\n"
"v_cvt_pk_bf16_f32 v155, v134, v135\n"
"buffer_store_dwordx4 v[152:155], v1, s[24:27], s16 offen offset:1024 sc1\n"
"s_waitcnt vmcnt(21)\n"
"ds_write_b128 v39, v[160:163] offset:0\n"
"v_pk_fma_f32 v[140:141], v[140:141], v[182:183], v[182:183] op_sel:[0,0,0] op_sel_hi:[1,0,0]\n"
"v_pk_mul_f32 v[112:113], v[80:81], v[80:81]\n"
"v_rcp_f32 v136, v136\n"
"v_rcp_f32 v137, v137\n"
"v_pk_fma_f32 v[142:143], v[142:143], v[182:183], v[182:183] op_sel:[0,0,0] op_sel_hi:[1,0,0]\n"
"buffer_load_dwordx4 v3, s[28:31], s64 offen offset:1024 lds\n"
"v_pk_mul_f32 v[114:115], v[82:83], v[82:83]\n"
"v_pk_mul_f32 v[116:117], v[84:85], v[84:85]\n"
"v_rcp_f32 v138, v138\n"
"v_rcp_f32 v139, v139\n"
"v_pk_mul_f32 v[118:119], v[86:87], v[86:87]\n"
"v_pk_fma_f32 v[112:113], v[112:113], v[176:177], v[176:177] op_sel:[0,0,1] op_sel_hi:[1,0,1]\n"
"v_pk_fma_f32 v[114:115], v[114:115], v[176:177], v[176:177] op_sel:[0,0,1] op_sel_hi:[1,0,1]\n"
"ds_write_b128 v39, v[168:171] offset:1024\n"
"v_rcp_f32 v140, v140\n"
"v_rcp_f32 v141, v141\n"
"v_pk_fma_f32 v[116:117], v[116:117], v[176:177], v[176:177] op_sel:[0,0,1] op_sel_hi:[1,0,1]\n"
"v_pk_fma_f32 v[118:119], v[118:119], v[176:177], v[176:177] op_sel:[0,0,1] op_sel_hi:[1,0,1]\n"
"v_pk_mul_f32 v[112:113], v[112:113], v[80:81]\n"
"v_rcp_f32 v142, v142\n"
"v_rcp_f32 v143, v143\n"
"buffer_load_dwordx4 v4, s[28:31], s64 offen offset:2048 lds\n"
"v_pk_mul_f32 v[114:115], v[114:115], v[82:83]\n"
"v_pk_mul_f32 v[116:117], v[116:117], v[84:85]\n"
"v_pk_mul_f32 v[118:119], v[118:119], v[86:87]\n"
"v_exp_f32 v128, v112\n"
"v_exp_f32 v129, v113\n"
"s_waitcnt lgkmcnt(0)\n"
"s_barrier\n"
"ds_read_b64 v[164:165], v39 offset:16\n"
"ds_read_b64 v[172:173], v39 offset:1040\n"
"v_pk_mul_f32 v[136:137], v[136:137], v[72:73]\n"
"v_pk_mul_f32 v[138:139], v[138:139], v[74:75]\n"
"v_pk_mul_f32 v[140:141], v[140:141], v[76:77]\n"
"v_exp_f32 v130, v114\n"
"v_exp_f32 v131, v115\n"
"v_pk_mul_f32 v[142:143], v[142:143], v[78:79]\n"
"ds_read_b64_tr_b16 a[28:29], v7 offset:34816\n"
"ds_read_b64_tr_b16 a[26:27], v6 offset:35840\n"
"ds_read_b64_tr_b16 a[30:31], v7 offset:35840\n"
"buffer_load_dwordx4 v5, s[28:31], s64 offen offset:3072 lds\n"
"v_pk_fma_f32 v[128:129], v[128:129], v[178:179], v[178:179] op_sel:[0,0,0] op_sel_hi:[1,0,0]\n"
"v_cvt_pk_bf16_f32 v156, v136, v137\n"
"v_exp_f32 v132, v116\n"
"v_exp_f32 v133, v117\n"
"v_cvt_pk_bf16_f32 v157, v138, v139\n"
"v_pk_fma_f32 v[130:131], v[130:131], v[178:179], v[178:179] op_sel:[0,0,0] op_sel_hi:[1,0,0]\n"
"s_waitcnt lgkmcnt(4)\n"
"v_alignbit_b32 v224, v161, v160, 16\n"
"v_alignbit_b32 v225, v162, v161, 16\n"
"v_alignbit_b32 v226, v163, v162, 16\n"
"v_alignbit_b32 v227, v164, v163, 16\n"
"ds_write_b128 v39, v[224:227] offset:4512\n"
"v_cvt_pk_bf16_f32 v158, v140, v141\n"
"v_exp_f32 v134, v118\n"
"v_exp_f32 v135, v119\n"
"s_waitcnt lgkmcnt(1)\n"
"s_add_u32 m0, s78, 32768\n"
"s_add_u32 s65, s65, 32768\n"
"buffer_load_dwordx4 v2, s[28:31], s65 offen offset:0 lds\n"
"v_cvt_pk_bf16_f32 v159, v142, v143\n"
"buffer_store_dwordx4 v[156:159], v1, s[24:27], s16 offen offset:3072 sc1\n"
"v_pk_fma_f32 v[132:133], v[132:133], v[178:179], v[178:179] op_sel:[0,0,0] op_sel_hi:[1,0,0]\n"
"v_pk_mul_f32 v[120:121], v[88:89], v[88:89]\n"
"v_rcp_f32 v128, v128\n"
"v_rcp_f32 v129, v129\n"
"v_pk_fma_f32 v[134:135], v[134:135], v[178:179], v[178:179] op_sel:[0,0,0] op_sel_hi:[1,0,0]\n"
"v_pk_mul_f32 v[122:123], v[90:91], v[90:91]\n"
"v_pk_mul_f32 v[124:125], v[92:93], v[92:93]\n"
"v_mov_b32 v228, v161\n"
"v_mov_b32 v229, v162\n"
"v_mov_b32 v230, v163\n"
"v_mov_b32 v231, v164\n"
"ds_write_b128 v39, v[228:231] offset:9024\n"
"v_rcp_f32 v130, v130\n"
"buffer_load_dwordx4 v3, s[28:31], s65 offen offset:1024 lds\n"
"v_rcp_f32 v131, v131\n"
"v_pk_mul_f32 v[126:127], v[94:95], v[94:95]\n"
"v_pk_fma_f32 v[120:121], v[120:121], v[176:177], v[176:177] op_sel:[0,0,1] op_sel_hi:[1,0,1]\n"
"v_pk_fma_f32 v[122:123], v[122:123], v[176:177], v[176:177] op_sel:[0,0,1] op_sel_hi:[1,0,1]\n"
"v_rcp_f32 v132, v132\n"
"v_rcp_f32 v133, v133\n"
"v_pk_fma_f32 v[124:125], v[124:125], v[176:177], v[176:177] op_sel:[0,0,1] op_sel_hi:[1,0,1]\n"
"v_pk_fma_f32 v[126:127], v[126:127], v[176:177], v[176:177] op_sel:[0,0,1] op_sel_hi:[1,0,1]\n"
"v_pk_mul_f32 v[120:121], v[120:121], v[88:89]\n"
"v_rcp_f32 v134, v134\n"
"v_rcp_f32 v135, v135\n"
"v_alignbit_b32 v232, v162, v161, 16\n"
"v_alignbit_b32 v233, v163, v162, 16\n"
"v_alignbit_b32 v234, v164, v163, 16\n"
"v_alignbit_b32 v235, v165, v164, 16\n"
"buffer_load_dwordx4 v4, s[28:31], s65 offen offset:2048 lds\n"
"ds_write_b128 v39, v[232:235] offset:13536\n"
"v_pk_mul_f32 v[122:123], v[122:123], v[90:91]\n"
"v_pk_mul_f32 v[124:125], v[124:125], v[92:93]\n"
"v_pk_mul_f32 v[126:127], v[126:127], v[94:95]\n"
"v_exp_f32 v136, v120\n"
"v_exp_f32 v137, v121\n"
"v_pk_mul_f32 v[128:129], v[128:129], v[80:81]\n"
"v_pk_mul_f32 v[130:131], v[130:131], v[82:83]\n"
"v_pk_mul_f32 v[132:133], v[132:133], v[84:85]\n"
"v_exp_f32 v138, v122\n"
"v_exp_f32 v139, v123\n"
"v_pk_mul_f32 v[134:135], v[134:135], v[86:87]\n"
"v_pk_fma_f32 v[136:137], v[136:137], v[178:179], v[178:179] op_sel:[0,0,0] op_sel_hi:[1,0,0]\n"
"v_alignbit_b32 v224, v169, v168, 16\n"
"buffer_load_dwordx4 v5, s[28:31], s65 offen offset:3072 lds\n"
"v_alignbit_b32 v225, v170, v169, 16\n"
"v_alignbit_b32 v226, v171, v170, 16\n"
"v_alignbit_b32 v227, v172, v171, 16\n"
"ds_write_b128 v39, v[224:227] offset:5536\n"
"v_cvt_pk_bf16_f32 v192, v128, v129\n"
"v_exp_f32 v140, v124\n"
"v_exp_f32 v141, v125\n"
"v_cvt_pk_bf16_f32 v193, v130, v131\n"
"v_pk_fma_f32 v[138:139], v[138:139], v[178:179], v[178:179] op_sel:[0,0,0] op_sel_hi:[1,0,0]\n"
"v_cvt_pk_bf16_f32 v194, v132, v133\n"
"v_exp_f32 v142, v126\n"
"v_exp_f32 v143, v127\n"
"v_cvt_pk_bf16_f32 v195, v134, v135\n"
"buffer_store_dwordx4 v[192:195], v1, s[24:27], s17 offen offset:0 sc1\n"
"v_pk_fma_f32 v[140:141], v[140:141], v[178:179], v[178:179] op_sel:[0,0,0] op_sel_hi:[1,0,0]\n"
"v_pk_mul_f32 v[112:113], v[96:97], v[96:97]\n"
"v_mov_b32 v228, v169\n"
"v_mov_b32 v229, v170\n"
"v_mov_b32 v230, v171\n"
"v_mov_b32 v231, v172\n"
"ds_write_b128 v39, v[228:231] offset:10048\n"
"v_rcp_f32 v136, v136\n"
"v_rcp_f32 v137, v137\n"
"v_pk_fma_f32 v[142:143], v[142:143], v[178:179], v[178:179] op_sel:[0,0,0] op_sel_hi:[1,0,0]\n"
"v_pk_mul_f32 v[114:115], v[98:99], v[98:99]\n"
"v_pk_mul_f32 v[116:117], v[100:101], v[100:101]\n"
"v_rcp_f32 v138, v138\n"
"v_rcp_f32 v139, v139\n"
"v_pk_mul_f32 v[118:119], v[102:103], v[102:103]\n"
"v_pk_fma_f32 v[112:113], v[112:113], v[180:181], v[180:181] op_sel:[0,0,1] op_sel_hi:[1,0,1]\n"
"v_pk_fma_f32 v[114:115], v[114:115], v[180:181], v[180:181] op_sel:[0,0,1] op_sel_hi:[1,0,1]\n"
"v_rcp_f32 v140, v140\n"
"v_rcp_f32 v141, v141\n"
"v_alignbit_b32 v232, v170, v169, 16\n"
"v_alignbit_b32 v233, v171, v170, 16\n"
"v_alignbit_b32 v234, v172, v171, 16\n"
"v_alignbit_b32 v235, v173, v172, 16\n"
"ds_write_b128 v39, v[232:235] offset:14560\n"
"v_pk_fma_f32 v[116:117], v[116:117], v[180:181], v[180:181] op_sel:[0,0,1] op_sel_hi:[1,0,1]\n"
"v_pk_fma_f32 v[118:119], v[118:119], v[180:181], v[180:181] op_sel:[0,0,1] op_sel_hi:[1,0,1]\n"
"v_pk_mul_f32 v[112:113], v[112:113], v[96:97]\n"
"v_rcp_f32 v142, v142\n"
"v_rcp_f32 v143, v143\n"
"v_pk_mul_f32 v[114:115], v[114:115], v[98:99]\n"
"v_pk_mul_f32 v[116:117], v[116:117], v[100:101]\n"
"v_pk_mul_f32 v[118:119], v[118:119], v[102:103]\n"
"v_exp_f32 v128, v112\n"
"v_exp_f32 v129, v113\n"
"v_pk_mul_f32 v[136:137], v[136:137], v[88:89]\n"
"v_pk_mul_f32 v[138:139], v[138:139], v[90:91]\n"
"v_pk_mul_f32 v[140:141], v[140:141], v[92:93]\n"
"v_exp_f32 v130, v114\n"
"v_exp_f32 v131, v115\n"
"v_pk_mul_f32 v[142:143], v[142:143], v[94:95]\n"
"v_pk_fma_f32 v[128:129], v[128:129], v[182:183], v[182:183] op_sel:[0,0,0] op_sel_hi:[1,0,0]\n"
"v_cvt_pk_bf16_f32 v196, v136, v137\n"
"v_exp_f32 v132, v116\n"
"v_exp_f32 v133, v117\n"
"v_cvt_pk_bf16_f32 v197, v138, v139\n"
"v_pk_fma_f32 v[130:131], v[130:131], v[182:183], v[182:183] op_sel:[0,0,0] op_sel_hi:[1,0,0]\n"
"v_cvt_pk_bf16_f32 v198, v140, v141\n"
"v_exp_f32 v134, v118\n"
"v_exp_f32 v135, v119\n"
"v_cvt_pk_bf16_f32 v199, v142, v143\n"
"buffer_store_dwordx4 v[196:199], v1, s[24:27], s17 offen offset:2048 sc1\n"
"v_pk_fma_f32 v[132:133], v[132:133], v[182:183], v[182:183] op_sel:[0,0,0] op_sel_hi:[1,0,0]\n"
"v_pk_mul_f32 v[120:121], v[104:105], v[104:105]\n"
"v_rcp_f32 v128, v128\n"
"v_rcp_f32 v129, v129\n"
"v_pk_fma_f32 v[134:135], v[134:135], v[182:183], v[182:183] op_sel:[0,0,0] op_sel_hi:[1,0,0]\n"
"v_pk_mul_f32 v[122:123], v[106:107], v[106:107]\n"
"v_pk_mul_f32 v[124:125], v[108:109], v[108:109]\n"
"v_rcp_f32 v130, v130\n"
"v_rcp_f32 v131, v131\n"
"v_pk_mul_f32 v[126:127], v[110:111], v[110:111]\n"
"v_pk_fma_f32 v[120:121], v[120:121], v[180:181], v[180:181] op_sel:[0,0,1] op_sel_hi:[1,0,1]\n"
"v_pk_fma_f32 v[122:123], v[122:123], v[180:181], v[180:181] op_sel:[0,0,1] op_sel_hi:[1,0,1]\n"
"v_rcp_f32 v132, v132\n"
"v_rcp_f32 v133, v133\n"
"v_pk_fma_f32 v[124:125], v[124:125], v[180:181], v[180:181] op_sel:[0,0,1] op_sel_hi:[1,0,1]\n"
"v_pk_fma_f32 v[126:127], v[126:127], v[180:181], v[180:181] op_sel:[0,0,1] op_sel_hi:[1,0,1]\n"
"v_pk_mul_f32 v[120:121], v[120:121], v[104:105]\n"
"v_rcp_f32 v134, v134\n"
"v_rcp_f32 v135, v135\n"
"v_pk_mul_f32 v[122:123], v[122:123], v[106:107]\n"
"v_pk_mul_f32 v[124:125], v[124:125], v[108:109]\n"
"v_pk_mul_f32 v[126:127], v[126:127], v[110:111]\n"
"v_exp_f32 v136, v120\n"
"v_exp_f32 v137, v121\n"
"v_pk_mul_f32 v[128:129], v[128:129], v[96:97]\n"
"v_pk_mul_f32 v[130:131], v[130:131], v[98:99]\n"
"v_pk_mul_f32 v[132:133], v[132:133], v[100:101]\n"
"v_exp_f32 v138, v122\n"
"v_exp_f32 v139, v123\n"
"v_pk_mul_f32 v[134:135], v[134:135], v[102:103]\n"
"v_pk_fma_f32 v[136:137], v[136:137], v[182:183], v[182:183] op_sel:[0,0,0] op_sel_hi:[1,0,0]\n"
"v_cvt_pk_bf16_f32 v200, v128, v129\n"
"v_exp_f32 v140, v124\n"
"v_exp_f32 v141, v125\n"
"v_cvt_pk_bf16_f32 v201, v130, v131\n"
"v_pk_fma_f32 v[138:139], v[138:139], v[182:183], v[182:183] op_sel:[0,0,0] op_sel_hi:[1,0,0]\n"
"v_cvt_pk_bf16_f32 v202, v132, v133\n"
"v_exp_f32 v142, v126\n"
"v_exp_f32 v143, v127\n"
"v_cvt_pk_bf16_f32 v203, v134, v135\n"
"buffer_store_dwordx4 v[200:203], v1, s[24:27], s17 offen offset:1024 sc1\n"
"v_pk_fma_f32 v[140:141], v[140:141], v[182:183], v[182:183] op_sel:[0,0,0] op_sel_hi:[1,0,0]\n"
"v_rcp_f32 v136, v136\n"
"v_rcp_f32 v137, v137\n"
"v_pk_fma_f32 v[142:143], v[142:143], v[182:183], v[182:183] op_sel:[0,0,0] op_sel_hi:[1,0,0]\n"
"v_rcp_f32 v138, v138\n"
"v_rcp_f32 v139, v139\n"
"v_rcp_f32 v140, v140\n"
"v_rcp_f32 v141, v141\n"
"v_rcp_f32 v142, v142\n"
"v_rcp_f32 v143, v143\n"
"v_pk_mul_f32 v[136:137], v[136:137], v[104:105]\n"
"v_pk_mul_f32 v[138:139], v[138:139], v[106:107]\n"
"v_pk_mul_f32 v[140:141], v[140:141], v[108:109]\n"
"v_pk_mul_f32 v[142:143], v[142:143], v[110:111]\n"
"v_cvt_pk_bf16_f32 v204, v136, v137\n"
"v_cvt_pk_bf16_f32 v205, v138, v139\n"
"v_cvt_pk_bf16_f32 v206, v140, v141\n"
"v_cvt_pk_bf16_f32 v207, v142, v143\n"
"LALT:\n"
"s_waitcnt vmcnt(6)\n"
"s_mov_b64 exec, 1\n"
"buffer_store_dword v18, off, s[24:27], s18 sc1\n"
"s_mov_b64 exec, -1\n"
"buffer_store_dwordx4 v[204:207], v1, s[24:27], s17 offen offset:3072 sc1\n"
"LJOIN:\n"
"v_add_u32 v47, 2048, v13\n"
"s_waitcnt lgkmcnt(0)\n"
"s_barrier\n"
"ds_read_b128 v[48:51], v12 offset:3584\n"
"ds_read_b64 v[80:81], v13 offset:0\n"
"ds_read_b64 v[82:83], v13 offset:8\n"
"ds_read_b128 v[52:55], v12 offset:3072\n"
"ds_read_b64 v[84:85], v13 offset:512\n"
"ds_read_b64 v[86:87], v13 offset:520\n"
"ds_read_b128 v[56:59], v12 offset:2560\n"
"ds_read_b64 v[88:89], v13 offset:1024\n"
"ds_read_b64 v[90:91], v13 offset:1032\n"
"s_waitcnt lgkmcnt(6)\n"
"v_mfma_f32_16x16x32_bf16 v[224:227], v[48:51], v[80:83], 0\n"
"ds_read_b128 v[60:63], v12 offset:2048\n"
"ds_read_b64 v[92:93], v13 offset:1536\n"
"s_waitcnt lgkmcnt(5)\n"
"v_mfma_f32_16x16x32_bf16 v[228:231], v[52:55], v[80:83], 0\n"
"ds_read_b64 v[94:95], v13 offset:1544\n"
"ds_read_b128 v[64:67], v12 offset:1536\n"
"v_mfma_f32_16x16x32_bf16 v[228:231], v[48:51], v[84:87], v[228:231]\n"
"ds_read_b64 v[96:97], v13 offset:2048\n"
"ds_read_b64 v[98:99], v13 offset:2056\n"
"v_mfma_f32_16x16x32_bf16 v[232:235], v[52:55], v[84:87], 0\n"
"ds_read_b128 v[68:71], v12 offset:1024\n"
"ds_read_b64 v[100:101], v13 offset:2560\n"
"s_waitcnt lgkmcnt(8)\n"
"v_mfma_f32_16x16x32_bf16 v[232:235], v[56:59], v[80:83], v[232:235]\n"
"ds_read_b64 v[102:103], v13 offset:2568\n"
"ds_read_b128 v[72:75], v12 offset:512\n"
"v_mfma_f32_16x16x32_bf16 v[232:235], v[48:51], v[88:91], v[232:235]\n"
"ds_read_b64 v[104:105], v13 offset:3072\n"
"ds_read_b64 v[106:107], v13 offset:3080\n"
"v_mfma_f32_16x16x32_bf16 v[236:239], v[56:59], v[84:87], 0\n"
"ds_read_b128 v[76:79], v12 offset:0\n"
"ds_read_b64 v[108:109], v13 offset:3584\n"
"v_mfma_f32_16x16x32_bf16 v[236:239], v[52:55], v[88:91], v[236:239]\n"
"ds_read_b64 v[110:111], v13 offset:3592\n"
"v_mfma_f32_16x16x32_bf16 v[240:243], v[56:59], v[88:91], 0\n"
"s_waitcnt vmcnt(34)\n"
"ds_read_b64_tr_b16 a[32:33], v6 offset:36864\n"
"s_waitcnt lgkmcnt(13)\n"
"v_mfma_f32_16x16x32_bf16 v[236:239], v[60:63], v[80:83], v[236:239]\n"
"ds_read_b64_tr_b16 a[36:37], v7 offset:36864\n"
"v_mfma_f32_16x16x32_bf16 v[236:239], v[48:51], v[92:95], v[236:239]\n"
"ds_read_b128 v[112:115], v12 offset:3648\n"
"ds_read_b64 v[144:145], v13 offset:64\n"
"v_mfma_f32_16x16x32_bf16 v[240:243], v[60:63], v[84:87], v[240:243]\n"
"ds_read_b64 v[146:147], v13 offset:72\n"
"ds_read_b64_tr_b16 a[34:35], v6 offset:37888\n"
"v_mfma_f32_16x16x32_bf16 v[240:243], v[52:55], v[92:95], v[240:243]\n"
"ds_read_b128 v[116:119], v12 offset:3136\n"
"ds_read_b64_tr_b16 a[38:39], v7 offset:37888\n"
"v_mfma_f32_16x16x32_bf16 v[244:247], v[60:63], v[88:91], 0\n"
"ds_read_b64 v[148:149], v13 offset:576\n"
"ds_read_b64_tr_b16 a[40:41], v6 offset:38912\n"
"v_mfma_f32_16x16x32_bf16 v[244:247], v[56:59], v[92:95], v[244:247]\n"
"ds_read_b64 v[150:151], v13 offset:584\n"
"ds_read_b64_tr_b16 a[44:45], v7 offset:38912\n"
"v_mfma_f32_16x16x32_bf16 v[248:251], v[60:63], v[92:95], 0\n"
"ds_read_b128 v[120:123], v12 offset:2624\n"
"ds_read_b64_tr_b16 a[42:43], v6 offset:39936\n"
"s_waitcnt lgkmcnt(15)\n"
"v_mfma_f32_16x16x32_bf16 v[240:243], v[64:67], v[80:83], v[240:243]\n"
"ds_read_b64 v[152:153], v13 offset:1088\n"
"v_mfma_f32_16x16x32_bf16 v[240:243], v[48:51], v[96:99], v[240:243]\n"
"ds_read_b64 v[154:155], v13 offset:1096\n"
"ds_read_b64_tr_b16 a[46:47], v7 offset:39936\n"
"v_mfma_f32_16x16x32_bf16 v[244:247], v[64:67], v[84:87], v[244:247]\n"
"ds_read_b128 v[124:127], v12 offset:2112\n"
"s_waitcnt vmcnt(30)\n"
"v_mfma_f32_16x16x32_bf16 v[244:247], v[52:55], v[96:99], v[244:247]\n"
"ds_read_b64 v[156:157], v13 offset:1600\n"
"ds_read_b64_tr_b16 a[48:49], v8 offset:0\n"
"v_mfma_f32_16x16x32_bf16 v[248:251], v[64:67], v[88:91], v[248:251]\n"
"ds_read_b64 v[158:159], v13 offset:1608\n"
"ds_read_b64_tr_b16 a[52:53], v9 offset:0\n"
"v_mfma_f32_16x16x32_bf16 v[248:251], v[56:59], v[96:99], v[248:251]\n"
"ds_read_b128 v[128:131], v12 offset:1600\n"
"ds_read_b64_tr_b16 a[50:51], v8 offset:1024\n"
"v_mfma_f32_16x16x32_bf16 v[252:255], v[64:67], v[92:95], 0\n"
"ds_read_b64 v[160:161], v13 offset:2112\n"
"s_waitcnt lgkmcnt(8)\n"
"v_mfma_f32_16x16x32_bf16 v[252:255], v[60:63], v[96:99], v[252:255]\n"
"ds_read_b64 v[162:163], v13 offset:2120\n"
"s_add_u32 m0, s78, 36864\n"
"v_mfma_f32_16x16x32_bf16 v[244:247], v[68:71], v[80:83], v[244:247]\n"
"ds_read_b128 v[132:135], v12 offset:1088\n"
"v_mfma_f32_16x16x32_bf16 v[244:247], v[48:51], v[100:103], v[244:247]\n"
"ds_read_b64 v[164:165], v13 offset:2624\n"
"s_add_u32 s66, s66, 32768\n"
"v_mfma_f32_16x16x32_bf16 v[248:251], v[68:71], v[84:87], v[248:251]\n"
"ds_read_b64 v[166:167], v13 offset:2632\n"
"buffer_load_dwordx4 v2, s[28:31], s66 offen offset:0 lds\n"
"v_mfma_f32_16x16x32_bf16 v[248:251], v[52:55], v[100:103], v[248:251]\n"
"ds_read_b128 v[136:139], v12 offset:576\n"
"ds_read_b64_tr_b16 a[54:55], v9 offset:1024\n"
"v_mfma_f32_16x16x32_bf16 v[252:255], v[68:71], v[88:91], v[252:255]\n"
"ds_read_b64 v[168:169], v13 offset:3136\n"
"buffer_load_dwordx4 v3, s[28:31], s66 offen offset:1024 lds\n"
"v_mfma_f32_16x16x32_bf16 v[252:255], v[56:59], v[100:103], v[252:255]\n"
"ds_read_b64 v[170:171], v13 offset:3144\n"
"ds_read_b64_tr_b16 a[56:57], v8 offset:2048\n"
"v_mfma_f32_16x16x32_bf16 v[248:251], v[72:75], v[80:83], v[248:251]\n"
"ds_read_b128 v[140:143], v12 offset:64\n"
"v_mfma_f32_16x16x32_bf16 v[248:251], v[48:51], v[104:107], v[248:251]\n"
"ds_read_b64 v[172:173], v13 offset:3648\n"
"ds_read_b64_tr_b16 a[60:61], v9 offset:2048\n"
"v_mfma_f32_16x16x32_bf16 v[252:255], v[72:75], v[84:87], v[252:255]\n"
"ds_read_b64 v[174:175], v13 offset:3656\n"
"buffer_load_dwordx4 v4, s[28:31], s66 offen offset:2048 lds\n"
"v_mfma_f32_16x16x32_bf16 v[252:255], v[52:55], v[104:107], v[252:255]\n"
"v_mov_b32 v21, v18\n"
"v_mfma_f32_16x16x32_bf16 v[252:255], v[76:79], v[80:83], v[252:255]\n"
"ds_read_b64_tr_b16 a[58:59], v8 offset:3072\n"
"v_mfma_f32_16x16x32_bf16 v[252:255], v[48:51], v[108:111], v[252:255]\n"
"ds_read_b64_tr_b16 a[62:63], v9 offset:3072\n"
"buffer_load_dwordx4 v5, s[28:31], s66 offen offset:3072 lds\n"
"s_waitcnt lgkmcnt(2)\n"
"v_mfma_f32_16x16x32_bf16 v[224:227], v[112:115], v[144:147], v[224:227]\n"
"ds_read_b128 v[48:51], v12 offset:3712\n"
"v_mfma_f32_16x16x32_bf16 v[228:231], v[116:119], v[144:147], v[228:231]\n"
"ds_read_b64 v[80:81], v13 offset:128\n"
"s_waitcnt vmcnt(30)\n"
"v_mfma_f32_16x16x32_bf16 v[228:231], v[112:115], v[148:151], v[228:231]\n"
"ds_read_b64 v[82:83], v13 offset:136\n"
"ds_read_b64_tr_b16 a[64:65], v8 offset:4096\n"
"v_mfma_f32_16x16x32_bf16 v[232:235], v[116:119], v[148:151], v[232:235]\n"
"ds_read_b128 v[52:55], v12 offset:3200\n"
"ds_read_b64_tr_b16 a[68:69], v9 offset:4096\n"
"v_mfma_f32_16x16x32_bf16 v[232:235], v[120:123], v[144:147], v[232:235]\n"
"ds_read_b64_tr_b16 a[66:67], v8 offset:5120\n"
"s_waitcnt lgkmcnt(7)\n"
"v_mfma_f32_16x16x32_bf16 v[232:235], v[112:115], v[152:155], v[232:235]\n"
"ds_read_b64 v[84:85], v13 offset:640\n"
"s_add_u32 m0, s78, 65536\n"
"v_mfma_f32_16x16x32_bf16 v[236:239], v[120:123], v[148:151], v[236:239]\n"
"ds_read_b64 v[86:87], v13 offset:648\n"
"s_add_u32 s67, s67, 32768\n"
"v_mfma_f32_16x16x32_bf16 v[236:239], v[116:119], v[152:155], v[236:239]\n"
"ds_read_b128 v[56:59], v12 offset:2688\n"
"buffer_load_dwordx4 v2, s[28:31], s67 offen offset:0 lds\n"
"v_mfma_f32_16x16x32_bf16 v[240:243], v[120:123], v[152:155], v[240:243]\n"
"s_waitcnt vmcnt(5)\n"
"s_mov_b64 exec, 1\n"
"buffer_store_dword v18, off, s[24:27], s19 sc1\n"
"s_mov_b64 exec, -1\n"
"v_mfma_f32_16x16x32_bf16 v[236:239], v[124:127], v[144:147], v[236:239]\n"
"ds_read_b64 v[88:89], v13 offset:1152\n"
"ds_read_b64_tr_b16 a[70:71], v9 offset:5120\n"
"v_mfma_f32_16x16x32_bf16 v[236:239], v[112:115], v[156:159], v[236:239]\n"
"ds_read_b64 v[90:91], v13 offset:1160\n"
"buffer_load_dwordx4 v3, s[28:31], s67 offen offset:1024 lds\n"
"v_mfma_f32_16x16x32_bf16 v[240:243], v[124:127], v[148:151], v[240:243]\n"
"ds_read_b128 v[60:63], v12 offset:2176\n"
"ds_read_b64_tr_b16 a[72:73], v8 offset:6144\n"
"v_mfma_f32_16x16x32_bf16 v[240:243], v[116:119], v[156:159], v[240:243]\n"
"ds_read_b64 v[92:93], v13 offset:1664\n"
"buffer_load_dwordx4 v4, s[28:31], s67 offen offset:2048 lds\n"
"v_mfma_f32_16x16x32_bf16 v[244:247], v[124:127], v[152:155], v[244:247]\n"
"ds_read_b64_tr_b16 a[76:77], v9 offset:6144\n"
"ds_read_b64_tr_b16 a[74:75], v8 offset:7168\n"
"v_mfma_f32_16x16x32_bf16 v[244:247], v[120:123], v[156:159], v[244:247]\n"
"ds_read_b64 v[94:95], v13 offset:1672\n"
"ds_read_b64_tr_b16 a[78:79], v9 offset:7168\n"
"v_mfma_f32_16x16x32_bf16 v[248:251], v[124:127], v[156:159], v[248:251]\n"
"ds_read_b128 v[64:67], v12 offset:1664\n"
"v_mfma_f32_16x16x32_bf16 v[240:243], v[128:131], v[144:147], v[240:243]\n"
"ds_read_b64 v[96:97], v13 offset:2176\n"
"buffer_load_dwordx4 v5, s[28:31], s67 offen offset:3072 lds\n"
"v_mfma_f32_16x16x32_bf16 v[240:243], v[112:115], v[160:163], v[240:243]\n"
"ds_read_b64_tr_b16 a[80:81], v8 offset:32768\n"
"ds_read_b64_tr_b16 a[84:85], v9 offset:32768\n"
"v_mfma_f32_16x16x32_bf16 v[244:247], v[128:131], v[148:151], v[244:247]\n"
"ds_read_b64 v[98:99], v13 offset:2184\n"
"s_mov_b64 exec, 127\n"
"buffer_load_dword v21, v15, s[24:27], s1 offen sc1\n"
"s_mov_b64 exec, -1\n"
"v_mfma_f32_16x16x32_bf16 v[244:247], v[116:119], v[160:163], v[244:247]\n"
"ds_read_b128 v[68:71], v12 offset:1152\n"
"s_waitcnt lgkmcnt(6)\n"
"v_mfma_f32_16x16x32_bf16 v[248:251], v[128:131], v[152:155], v[248:251]\n"
"ds_read_b64 v[100:101], v13 offset:2688\n"
"s_add_u32 m0, s78, 69632\n"
"v_mfma_f32_16x16x32_bf16 v[248:251], v[120:123], v[160:163], v[248:251]\n"
"ds_read_b64 v[102:103], v13 offset:2696\n"
"s_add_u32 s68, s68, 32768\n"
"v_mfma_f32_16x16x32_bf16 v[252:255], v[128:131], v[156:159], v[252:255]\n"
"buffer_load_dwordx4 v2, s[28:31], s68 offen offset:0 lds\n"
"ds_read_b64_tr_b16 a[82:83], v8 offset:33792\n"
"v_mfma_f32_16x16x32_bf16 v[252:255], v[124:127], v[160:163], v[252:255]\n"
"ds_read_b128 v[72:75], v12 offset:640\n"
"ds_read_b64_tr_b16 a[86:87], v9 offset:33792\n"
"v_mfma_f32_16x16x32_bf16 v[244:247], v[132:135], v[144:147], v[244:247]\n"
"ds_read_b64 v[104:105], v13 offset:3200\n"
"buffer_load_dwordx4 v3, s[28:31], s68 offen offset:1024 lds\n"
"v_mfma_f32_16x16x32_bf16 v[244:247], v[112:115], v[164:167], v[244:247]\n"
"ds_read_b64 v[106:107], v13 offset:3208\n"
"ds_read_b64_tr_b16 a[88:89], v8 offset:34816\n"
"v_mfma_f32_16x16x32_bf16 v[248:251], v[132:135], v[148:151], v[248:251]\n"
"ds_read_b64_tr_b16 a[92:93], v9 offset:34816\n"
"ds_read_b64_tr_b16 a[90:91], v8 offset:35840\n"
"v_mfma_f32_16x16x32_bf16 v[248:251], v[116:119], v[164:167], v[248:251]\n"
"ds_read_b128 v[76:79], v12 offset:128\n"
"buffer_load_dwordx4 v4, s[28:31], s68 offen offset:2048 lds\n"
"v_mfma_f32_16x16x32_bf16 v[252:255], v[132:135], v[152:155], v[252:255]\n"
"ds_read_b64 v[108:109], v13 offset:3712\n"
"ds_read_b64_tr_b16 a[94:95], v9 offset:35840\n"
"v_mfma_f32_16x16x32_bf16 v[252:255], v[120:123], v[164:167], v[252:255]\n"
"ds_read_b64 v[110:111], v13 offset:3720\n"
"v_mfma_f32_16x16x32_bf16 v[248:251], v[136:139], v[144:147], v[248:251]\n"
"ds_read_b64_tr_b16 a[96:97], v8 offset:36864\n"
"ds_read_b64_tr_b16 a[100:101], v9 offset:36864\n"
"v_mfma_f32_16x16x32_bf16 v[248:251], v[112:115], v[168:171], v[248:251]\n"
"buffer_load_dwordx4 v5, s[28:31], s68 offen offset:3072 lds\n"
"ds_read_b64_tr_b16 a[98:99], v8 offset:37888\n"
"v_mfma_f32_16x16x32_bf16 v[252:255], v[136:139], v[148:151], v[252:255]\n"
"ds_read_b64_tr_b16 a[102:103], v9 offset:37888\n"
"ds_read_b64_tr_b16 a[104:105], v8 offset:38912\n"
"v_mfma_f32_16x16x32_bf16 v[252:255], v[116:119], v[168:171], v[252:255]\n"
"ds_read_b64_tr_b16 a[108:109], v9 offset:38912\n"
"s_waitcnt lgkmcnt(7)\n"
"v_mfma_f32_16x16x32_bf16 v[252:255], v[140:143], v[144:147], v[252:255]\n"
"s_add_u32 m0, s78, 98304\n"
"s_add_u32 s69, s69, 32768\n"
"buffer_load_dwordx4 v2, s[28:31], s69 offen offset:0 lds\n"
"v_mfma_f32_16x16x32_bf16 v[252:255], v[112:115], v[172:175], v[252:255]\n"
"ds_read_b64_tr_b16 a[106:107], v8 offset:39936\n"
"ds_read_b64_tr_b16 a[110:111], v9 offset:39936\n"
"s_waitcnt lgkmcnt(8)\n"
"v_mfma_f32_16x16x32_bf16 v[224:227], v[48:51], v[80:83], v[224:227]\n"
"ds_read_b128 v[112:115], v12 offset:3776\n"
"v_mfma_f32_16x16x32_bf16 v[228:231], v[52:55], v[80:83], v[228:231]\n"
"ds_read_b64 v[144:145], v13 offset:192\n"
"buffer_load_dwordx4 v3, s[28:31], s69 offen offset:1024 lds\n"
"v_mfma_f32_16x16x32_bf16 v[228:231], v[48:51], v[84:87], v[228:231]\n"
"ds_read_b64 v[146:147], v13 offset:200\n"
"v_mfma_f32_16x16x32_bf16 v[232:235], v[52:55], v[84:87], v[232:235]\n"
"ds_read_b128 v[116:119], v12 offset:3264\n"
"ds_read_b64_tr_b16 a[112:113], v10 offset:0\n"
"v_mfma_f32_16x16x32_bf16 v[232:235], v[56:59], v[80:83], v[232:235]\n"
"ds_read_b64_tr_b16 a[116:117], v11 offset:0\n"
"ds_read_b64_tr_b16 a[114:115], v10 offset:1024\n"
"v_mfma_f32_16x16x32_bf16 v[232:235], v[48:51], v[88:91], v[232:235]\n"
"ds_read_b64 v[148:149], v13 offset:704\n"
"buffer_load_dwordx4 v4, s[28:31], s69 offen offset:2048 lds\n"
"v_mfma_f32_16x16x32_bf16 v[236:239], v[56:59], v[84:87], v[236:239]\n"
"ds_read_b64 v[150:151], v13 offset:712\n"
"ds_read_b64_tr_b16 a[118:119], v11 offset:1024\n"
"v_mfma_f32_16x16x32_bf16 v[236:239], v[52:55], v[88:91], v[236:239]\n"
"ds_read_b128 v[120:123], v12 offset:2752\n"
"ds_read_b64_tr_b16 a[120:121], v10 offset:2048\n"
"v_mfma_f32_16x16x32_bf16 v[240:243], v[56:59], v[88:91], v[240:243]\n"
"buffer_load_dwordx4 v5, s[28:31], s69 offen offset:3072 lds\n"
"s_waitcnt lgkmcnt(12)\n"
"v_mfma_f32_16x16x32_bf16 v[236:239], v[60:63], v[80:83], v[236:239]\n"
"ds_read_b64 v[152:153], v13 offset:1216\n"
"s_add_u32 m0, s78, 102400\n"
"v_mfma_f32_16x16x32_bf16 v[236:239], v[48:51], v[92:95], v[236:239]\n"
"ds_read_b64 v[154:155], v13 offset:1224\n"
"s_add_u32 s70, s70, 32768\n"
"v_mfma_f32_16x16x32_bf16 v[240:243], v[60:63], v[84:87], v[240:243]\n"
"ds_read_b128 v[124:127], v12 offset:2240\n"
"ds_read_b64_tr_b16 a[124:125], v11 offset:2048\n"
"v_mfma_f32_16x16x32_bf16 v[240:243], v[52:55], v[92:95], v[240:243]\n"
"ds_read_b64 v[156:157], v13 offset:1728\n"
"buffer_load_dwordx4 v2, s[28:31], s70 offen offset:0 lds\n"
"v_mfma_f32_16x16x32_bf16 v[244:247], v[60:63], v[88:91], v[244:247]\n"
"ds_read_b64_tr_b16 a[122:123], v10 offset:3072\n"
"ds_read_b64_tr_b16 a[126:127], v11 offset:3072\n"
"v_mfma_f32_16x16x32_bf16 v[244:247], v[56:59], v[92:95], v[244:247]\n"
"ds_read_b64 v[158:159], v13 offset:1736\n"
"v_mfma_f32_16x16x32_bf16 v[248:251], v[60:63], v[92:95], v[248:251]\n"
"ds_read_b128 v[128:131], v12 offset:1728\n"
"buffer_load_dwordx4 v3, s[28:31], s70 offen offset:1024 lds\n"
"v_mfma_f32_16x16x32_bf16 v[240:243], v[64:67], v[80:83], v[240:243]\n"
"ds_read_b64 v[160:161], v13 offset:2240\n"
"s_waitcnt vmcnt(10)\n"
"v_mfma_f32_16x16x32_bf16 v[240:243], v[48:51], v[96:99], v[240:243]\n"
"v_cmp_ne_u32 vcc, v18, v21\n"
"s_cbranch_vccnz LS4\n"
"LB4:\n"
"v_mfma_f32_16x16x32_bf16 v[244:247], v[64:67], v[84:87], v[244:247]\n"
"ds_read_b64 v[162:163], v13 offset:2248\n"
"buffer_load_dwordx4 v[176:179], v17, s[24:27], s71 offen offset:0 sc1\n"
"v_mfma_f32_16x16x32_bf16 v[244:247], v[52:55], v[96:99], v[244:247]\n"
"ds_read_b128 v[132:135], v12 offset:1216\n"
"v_mfma_f32_16x16x32_bf16 v[248:251], v[64:67], v[88:91], v[248:251]\n"
"ds_read_b64 v[164:165], v13 offset:2752\n"
"v_mfma_f32_16x16x32_bf16 v[248:251], v[56:59], v[96:99], v[248:251]\n"
"ds_read_b64 v[166:167], v13 offset:2760\n"
"buffer_load_dwordx4 v[180:183], v17, s[24:27], s71 offen offset:2048 sc1\n"
"v_mfma_f32_16x16x32_bf16 v[252:255], v[64:67], v[92:95], v[252:255]\n"
"v_mfma_f32_16x16x32_bf16 v[252:255], v[60:63], v[96:99], v[252:255]\n"
"ds_read_b128 v[136:139], v12 offset:704\n"
"v_mfma_f32_16x16x32_bf16 v[244:247], v[68:71], v[80:83], v[244:247]\n"
"ds_read_b64 v[168:169], v13 offset:3264\n"
"buffer_load_dwordx4 v[184:187], v17, s[24:27], s72 offen offset:0 sc1\n"
"v_mfma_f32_16x16x32_bf16 v[244:247], v[48:51], v[100:103], v[244:247]\n"
"ds_read_b64 v[170:171], v13 offset:3272\n"
"v_mfma_f32_16x16x32_bf16 v[248:251], v[68:71], v[84:87], v[248:251]\n"
"v_mfma_f32_16x16x32_bf16 v[248:251], v[52:55], v[100:103], v[248:251]\n"
"ds_read_b128 v[140:143], v12 offset:192\n"
"buffer_load_dwordx4 v4, s[28:31], s70 offen offset:2048 lds\n"
"v_mfma_f32_16x16x32_bf16 v[252:255], v[68:71], v[88:91], v[252:255]\n"
"ds_read_b64 v[172:173], v13 offset:3776\n"
"v_mfma_f32_16x16x32_bf16 v[252:255], v[56:59], v[100:103], v[252:255]\n"
"ds_read_b64 v[174:175], v13 offset:3784\n"
"v_mfma_f32_16x16x32_bf16 v[248:251], v[72:75], v[80:83], v[248:251]\n"
"buffer_load_dwordx4 v[188:191], v17, s[24:27], s72 offen offset:2048 sc1\n"
"v_mfma_f32_16x16x32_bf16 v[248:251], v[48:51], v[104:107], v[248:251]\n"
"v_mov_b32 v21, v18\n"
"s_mov_b64 exec, 127\n"
"buffer_load_dword v21, v15, s[24:27], s2 offen sc1\n"
"s_mov_b64 exec, -1\n"
"v_mfma_f32_16x16x32_bf16 v[252:255], v[72:75], v[84:87], v[252:255]\n"
"v_mfma_f32_16x16x32_bf16 v[252:255], v[52:55], v[104:107], v[252:255]\n"
"buffer_load_dwordx4 v5, s[28:31], s70 offen offset:3072 lds\n"
"v_mfma_f32_16x16x32_bf16 v[252:255], v[76:79], v[80:83], v[252:255]\n"
"v_mfma_f32_16x16x32_bf16 v[252:255], v[48:51], v[108:111], v[252:255]\n"
"s_waitcnt lgkmcnt(0)\n"
"v_mfma_f32_16x16x32_bf16 v[224:227], v[112:115], v[144:147], v[224:227]\n"
"buffer_load_dwordx4 v[192:195], v17, s[24:27], s73 offen offset:0 sc1\n"
"v_mfma_f32_16x16x32_bf16 v[228:231], v[116:119], v[144:147], v[228:231]\n"
"v_mfma_f32_16x16x32_bf16 v[228:231], v[112:115], v[148:151], v[228:231]\n"
"ds_read_b64 v[48:49], v14 offset:1536\n"
"ds_read_b64 v[50:51], v14 offset:1024\n"
"v_mfma_f32_16x16x32_bf16 v[232:235], v[116:119], v[148:151], v[232:235]\n"
"buffer_load_dwordx4 v[196:199], v17, s[24:27], s73 offen offset:2048 sc1\n"
"ds_read_b64 v[52:53], v14 offset:512\n"
"v_mfma_f32_16x16x32_bf16 v[232:235], v[120:123], v[144:147], v[232:235]\n"
"ds_read_b64 v[54:55], v14 offset:0\n"
"v_mfma_f32_16x16x32_bf16 v[232:235], v[112:115], v[152:155], v[232:235]\n"
"v_lshlrev_b32 v22, 16, v22\n"
"v_mfma_f32_16x16x32_bf16 v[236:239], v[120:123], v[148:151], v[236:239]\n"
"v_mfma_f32_16x16x32_bf16 v[236:239], v[116:119], v[152:155], v[236:239]\n"
"buffer_load_dwordx4 v[200:203], v17, s[24:27], s74 offen offset:0 sc1\n"
"v_mfma_f32_16x16x32_bf16 v[240:243], v[120:123], v[152:155], v[240:243]\n"
"v_mfma_f32_16x16x32_bf16 v[236:239], v[124:127], v[144:147], v[236:239]\n"
"v_mfma_f32_16x16x32_bf16 v[236:239], v[112:115], v[156:159], v[236:239]\n"
"s_waitcnt lgkmcnt(0)\n"
"buffer_load_dwordx4 v[204:207], v17, s[24:27], s74 offen offset:2048 sc1\n"
"v_mfma_f32_16x16x32_bf16 v[240:243], v[124:127], v[148:151], v[240:243]\n"
"v_and_b32 v56, 0xffff0000, v49\n"
"v_lshlrev_b32 v57, 16, v49\n"
"v_mfma_f32_16x16x32_bf16 v[240:243], v[116:119], v[156:159], v[240:243]\n"
"v_and_b32 v58, 0xffff0000, v48\n"
"v_lshlrev_b32 v59, 16, v48\n"
"v_mfma_f32_16x16x32_bf16 v[244:247], v[124:127], v[152:155], v[244:247]\n"
"v_and_b32 v60, 0xffff0000, v51\n"
"v_lshlrev_b32 v61, 16, v51\n"
"v_mfma_f32_16x16x32_bf16 v[244:247], v[120:123], v[156:159], v[244:247]\n"
"v_and_b32 v62, 0xffff0000, v50\n"
"v_lshlrev_b32 v63, 16, v50\n"
"v_mfma_f32_16x16x32_bf16 v[248:251], v[124:127], v[156:159], v[248:251]\n"
"buffer_load_dwordx4 v[208:211], v17, s[24:27], s75 offen offset:0 sc1\n"
"v_mfma_f32_16x16x32_bf16 v[240:243], v[128:131], v[144:147], v[240:243]\n"
"v_and_b32 v64, 0xffff0000, v53\n"
"v_lshlrev_b32 v65, 16, v53\n"
"v_mfma_f32_16x16x32_bf16 v[240:243], v[112:115], v[160:163], v[240:243]\n"
"v_and_b32 v66, 0xffff0000, v52\n"
"v_lshlrev_b32 v67, 16, v52\n"
"v_mfma_f32_16x16x32_bf16 v[244:247], v[128:131], v[148:151], v[244:247]\n"
"v_and_b32 v68, 0xffff0000, v55\n"
"v_lshlrev_b32 v69, 16, v55\n"
"v_mfma_f32_16x16x32_bf16 v[244:247], v[116:119], v[160:163], v[244:247]\n"
"buffer_load_dwordx4 v[212:215], v17, s[24:27], s75 offen offset:2048 sc1\n"
"v_mfma_f32_16x16x32_bf16 v[248:251], v[128:131], v[152:155], v[248:251]\n"
"v_and_b32 v70, 0xffff0000, v54\n"
"v_lshlrev_b32 v71, 16, v54\n"
"v_mfma_f32_16x16x32_bf16 v[248:251], v[120:123], v[160:163], v[248:251]\n"
"s_mov_b32 s42, -1\n"
"s_mov_b32 s43, 0\n"
"v_mfma_f32_16x16x32_bf16 v[252:255], v[128:131], v[156:159], v[252:255]\n"
"v_mov_b32 v37, 0\n"
"v_mov_b32 v38, 0\n"
"v_mfma_f32_16x16x32_bf16 v[252:255], v[124:127], v[160:163], v[252:255]\n"
"buffer_load_dwordx4 v[216:219], v17, s[24:27], s76 offen offset:0 sc1\n"
"v_mfma_f32_16x16x32_bf16 v[244:247], v[132:135], v[144:147], v[244:247]\n"
"v_mov_b32 v39, 0\n"
"v_cndmask_b32 v36, 0, v36, s[42:43]\n"
"v_mfma_f32_16x16x32_bf16 v[244:247], v[112:115], v[164:167], v[244:247]\n"
"v_mov_b32 v40, 0x3f80\n"
"v_mov_b32 v41, 0\n"
"v_mfma_f32_16x16x32_bf16 v[248:251], v[132:135], v[148:151], v[248:251]\n"
"v_mov_b32 v42, 0\n"
"v_mov_b32 v43, 0\n"
"v_mfma_f32_16x16x32_bf16 v[248:251], v[116:119], v[164:167], v[248:251]\n"
"buffer_load_dwordx4 v[220:223], v17, s[24:27], s76 offen offset:2048 sc1\n"
"v_mfma_f32_16x16x32_bf16 v[252:255], v[132:135], v[152:155], v[252:255]\n"
"v_mfma_f32_16x16x32_bf16 v[252:255], v[120:123], v[164:167], v[252:255]\n"
"v_mfma_f32_16x16x32_bf16 v[248:251], v[136:139], v[144:147], v[248:251]\n"
"v_mfma_f32_16x16x32_bf16 v[248:251], v[112:115], v[168:171], v[248:251]\n"
"buffer_load_dwordx4 a[224:227], v17, s[24:27], s77 offen offset:0 sc1\n"
"v_mfma_f32_16x16x32_bf16 v[252:255], v[136:139], v[148:151], v[252:255]\n"
"v_mfma_f32_16x16x32_bf16 v[252:255], v[116:119], v[168:171], v[252:255]\n"
"v_mfma_f32_16x16x32_bf16 v[252:255], v[140:143], v[144:147], v[252:255]\n"
"v_mfma_f32_16x16x32_bf16 v[252:255], v[112:115], v[172:175], v[252:255]\n"
"buffer_load_dwordx4 a[228:231], v17, s[24:27], s77 offen offset:2048 sc1\n"
"s_bitcmp1_b32 s5, 0\n"
"s_cbranch_scc1 LCX1\n"
"s_nop 5\n"
"ds_write_b128 v19, v[240:243] offset:0\n"
"v_pk_fma_f32 v[124:125], v[56:57], v[22:23], v[224:225] op_sel:[0,0,0] op_sel_hi:[1,0,1]\n"
"v_pk_fma_f32 v[126:127], v[58:59], v[22:23], v[226:227] op_sel:[0,0,0] op_sel_hi:[1,0,1]\n"
"ds_write_b128 v19, v[244:247] offset:1024\n"
"v_pk_fma_f32 v[128:129], v[60:61], v[22:23], v[228:229] op_sel:[0,0,0] op_sel_hi:[1,0,1]\n"
"v_pk_fma_f32 v[130:131], v[62:63], v[22:23], v[230:231] op_sel:[0,0,0] op_sel_hi:[1,0,1]\n"
"ds_write_b128 v19, v[248:251] offset:2048\n"
"v_pk_fma_f32 v[132:133], v[64:65], v[22:23], v[232:233] op_sel:[0,0,0] op_sel_hi:[1,0,1]\n"
"v_pk_fma_f32 v[134:135], v[66:67], v[22:23], v[234:235] op_sel:[0,0,0] op_sel_hi:[1,0,1]\n"
"ds_write_b128 v19, v[252:255] offset:3072\n"
"v_pk_fma_f32 v[136:137], v[68:69], v[22:23], v[236:237] op_sel:[0,0,0] op_sel_hi:[1,0,1]\n"
"v_pk_fma_f32 v[138:139], v[70:71], v[22:23], v[238:239] op_sel:[0,0,0] op_sel_hi:[1,0,1]\n"
"LCX2:\n"
"v_mfma_f32_32x32x16_bf16 v[48:63], v[36:39], v[40:43], 0\n"
"s_waitcnt vmcnt(12)\n"
"v_mfma_f32_32x32x16_bf16 v[64:79], a[0:3], v[176:179], 0\n"
"s_waitcnt lgkmcnt(0)\n"
"s_barrier\n"
"ds_read_b128 v[140:143], v20 offset:0\n"
"v_mfma_f32_32x32x16_bf16 v[96:111], a[4:7], v[176:179], 0\n"
"ds_read_b128 v[144:147], v20 offset:1024\n"
"ds_read_b128 v[148:151], v20 offset:2048\n"
"ds_read_b128 v[152:155], v20 offset:3072\n"
"v_mfma_f32_32x32x16_bf16 v[96:111], a[12:15], v[180:183], v[96:111]\n"
"s_waitcnt vmcnt(11)\n"
"v_cmp_ne_u32 vcc, v18, v21\n"
"s_cbranch_vccnz LS5\n"
"LB5:\n"
"v_add_u32 v17, 4096, v17\n"
"v_mfma_f32_32x32x16_bf16 v[64:79], a[8:11], v[180:183], v[64:79]\n"
"buffer_load_dwordx4 a[232:235], v17, s[24:27], s71 offen offset:0 sc1\n"
"ds_read_b64_tr_b16 a[128:129], v6 offset:32768\n"
"ds_read_b64_tr_b16 a[132:133], v7 offset:32768\n"
"v_mfma_f32_32x32x16_bf16 v[64:79], a[16:19], v[184:187], v[64:79]\n"
"buffer_load_dwordx4 a[236:239], v16, s[24:27], s71 offen offset:0 sc1\n"
"s_mov_b32 s49, 16777216\n"
"s_waitcnt lgkmcnt(2)\n"
"v_add_f32 v124, v124, v140\n"
"v_mfma_f32_32x32x16_bf16 v[96:111], a[20:23], v[184:187], v[96:111]\n"
"v_add_f32 v125, v125, v141\n"
"v_add_f32 v126, v126, v142\n"
"v_add_f32 v127, v127, v143\n"
"v_add_f32 v128, v128, v144\n"
"v_add_f32 v129, v129, v145\n"
"v_add_f32 v130, v130, v146\n"
"v_mfma_f32_32x32x16_bf16 v[96:111], a[28:31], v[188:191], v[96:111]\n"
"buffer_load_dwordx4 v[120:123], v46, s[24:27], s49 offen sc1\n"
"v_add_f32 v131, v131, v147\n"
"v_add_f32 v132, v132, v148\n"
"v_add_f32 v133, v133, v149\n"
"v_add_f32 v134, v134, v150\n"
"v_mfma_f32_32x32x16_bf16 v[64:79], a[24:27], v[188:191], v[64:79]\n"
"v_add_f32 v135, v135, v151\n"
"v_add_f32 v136, v136, v152\n"
"v_add_f32 v137, v137, v153\n"
"v_add_f32 v138, v138, v154\n"
"v_add_f32 v139, v139, v155\n"
"v_cvt_pk_bf16_f32 v156, v124, v125\n"
"s_waitcnt vmcnt(9)\n"
"v_mfma_f32_32x32x16_bf16 v[64:79], a[32:35], v[192:195], v[64:79]\n"
"v_cvt_pk_bf16_f32 v157, v126, v127\n"
"v_cvt_pk_bf16_f32 v158, v128, v129\n"
"v_cvt_pk_bf16_f32 v159, v130, v131\n"
"v_cvt_pk_bf16_f32 v160, v132, v133\n"
"v_cvt_pk_bf16_f32 v161, v134, v135\n"
"v_mfma_f32_32x32x16_bf16 v[96:111], a[36:39], v[192:195], v[96:111]\n"
"v_cvt_pk_bf16_f32 v162, v136, v137\n"
"v_cvt_pk_bf16_f32 v163, v138, v139\n"
"ds_read_b64_tr_b16 a[130:131], v6 offset:33792\n"
"v_mfma_f32_32x32x16_bf16 v[96:111], a[44:47], v[196:199], v[96:111]\n"
"buffer_store_dwordx2 v[156:157], v44, s[24:27], s80 offen offset:0 sc1\n"
"buffer_store_dwordx2 v[158:159], v44, s[24:27], s80 offen offset:512 sc1\n"
"buffer_store_dwordx2 v[160:161], v44, s[24:27], s80 offen offset:1024 sc1\n"
"buffer_store_dwordx2 v[162:163], v44, s[24:27], s80 offen offset:1536 sc1\n"
"v_mfma_f32_32x32x16_bf16 v[64:79], a[40:43], v[196:199], v[64:79]\n"
"buffer_load_dwordx4 a[240:243], v17, s[24:27], s71 offen offset:2048 sc1\n"
"ds_read_b64_tr_b16 a[134:135], v7 offset:33792\n"
"ds_read_b64_tr_b16 a[136:137], v6 offset:34816\n"
"v_mfma_f32_32x32x16_bf16 v[64:79], a[48:51], v[200:203], v[64:79]\n"
"ds_read_b64_tr_b16 a[140:141], v7 offset:34816\n"
"buffer_load_dwordx4 a[244:247], v16, s[24:27], s71 offen offset:2048 sc1\n"
"ds_read_b64_tr_b16 a[138:139], v6 offset:35840\n"
"v_mfma_f32_32x32x16_bf16 v[96:111], a[52:55], v[200:203], v[96:111]\n"
"ds_read_b64_tr_b16 a[142:143], v7 offset:35840\n"
"ds_read_b64_tr_b16 a[144:145], v6 offset:36864\n"
"ds_read_b64_tr_b16 a[148:149], v7 offset:36864\n"
"v_mfma_f32_32x32x16_bf16 v[96:111], a[60:63], v[204:207], v[96:111]\n"
"ds_read_b64_tr_b16 a[146:147], v6 offset:37888\n"
"buffer_load_dwordx4 a[248:251], v17, s[24:27], s72 offen offset:0 sc1\n"
"ds_read_b64_tr_b16 a[150:151], v7 offset:37888\n"
"v_mfma_f32_32x32x16_bf16 v[64:79], a[56:59], v[204:207], v[64:79]\n"
"ds_read_b64_tr_b16 a[152:153], v6 offset:38912\n"
"ds_read_b64_tr_b16 a[156:157], v7 offset:38912\n"
"ds_read_b64_tr_b16 a[154:155], v6 offset:39936\n"
"s_waitcnt vmcnt(12)\n"
"v_mfma_f32_32x32x16_bf16 v[64:79], a[64:67], v[208:211], v[64:79]\n"
"buffer_load_dwordx4 a[252:255], v16, s[24:27], s72 offen offset:0 sc1\n"
"ds_read_b64_tr_b16 a[158:159], v7 offset:39936\n"
"v_mfma_f32_32x32x16_bf16 v[96:111], a[68:71], v[208:211], v[96:111]\n"
"ds_read_b64_tr_b16 a[160:161], v8 offset:0\n"
"ds_read_b64_tr_b16 a[164:165], v9 offset:0\n"
"ds_read_b64_tr_b16 a[162:163], v8 offset:1024\n"
"v_mfma_f32_32x32x16_bf16 v[96:111], a[76:79], v[212:215], v[96:111]\n"
"buffer_load_dwordx4 v[224:227], v17, s[24:27], s72 offen offset:2048 sc1\n"
"ds_read_b64_tr_b16 a[166:167], v9 offset:1024\n"
"ds_read_b64_tr_b16 a[168:169], v8 offset:2048\n"
"v_mfma_f32_32x32x16_bf16 v[64:79], a[72:75], v[212:215], v[64:79]\n"
"ds_read_b64_tr_b16 a[172:173], v9 offset:2048\n"
"ds_read_b64_tr_b16 a[170:171], v8 offset:3072\n"
"ds_read_b64_tr_b16 a[174:175], v9 offset:3072\n"
"v_mfma_f32_32x32x16_bf16 v[64:79], a[80:83], v[216:219], v[64:79]\n"
"buffer_load_dwordx4 v[228:231], v16, s[24:27], s72 offen offset:2048 sc1\n"
"ds_read_b64_tr_b16 a[176:177], v8 offset:4096\n"
"ds_read_b64_tr_b16 a[180:181], v9 offset:4096\n"
"v_mfma_f32_32x32x16_bf16 v[96:111], a[84:87], v[216:219], v[96:111]\n"
"ds_read_b64_tr_b16 a[178:179], v8 offset:5120\n"
"ds_read_b64_tr_b16 a[182:183], v9 offset:5120\n"
"ds_read_b64_tr_b16 a[184:185], v8 offset:6144\n"
"v_mfma_f32_32x32x16_bf16 v[96:111], a[92:95], v[220:223], v[96:111]\n"
"buffer_load_dwordx4 v[232:235], v17, s[24:27], s73 offen offset:0 sc1\n"
"ds_read_b64_tr_b16 a[188:189], v9 offset:6144\n"
"ds_read_b64_tr_b16 a[186:187], v8 offset:7168\n"
"v_mfma_f32_32x32x16_bf16 v[64:79], a[88:91], v[220:223], v[64:79]\n"
"ds_read_b64_tr_b16 a[190:191], v9 offset:7168\n"
"ds_read_b64_tr_b16 a[192:193], v8 offset:32768\n"
"buffer_load_dwordx4 v[236:239], v16, s[24:27], s73 offen offset:0 sc1\n"
"s_waitcnt vmcnt(13)\n"
"v_mfma_f32_32x32x16_bf16 v[64:79], a[96:99], a[224:227], v[64:79]\n"
"ds_read_b64_tr_b16 a[196:197], v9 offset:32768\n"
"ds_read_b64_tr_b16 a[194:195], v8 offset:33792\n"
"ds_read_b64_tr_b16 a[198:199], v9 offset:33792\n"
"v_mfma_f32_32x32x16_bf16 v[96:111], a[100:103], a[224:227], v[96:111]\n"
"ds_read_b64_tr_b16 a[200:201], v8 offset:34816\n"
"ds_read_b64_tr_b16 a[204:205], v9 offset:34816\n"
"buffer_load_dwordx4 v[240:243], v17, s[24:27], s73 offen offset:2048 sc1\n"
"s_waitcnt vmcnt(13)\n"
"v_mfma_f32_32x32x16_bf16 v[96:111], a[108:111], a[228:231], v[96:111]\n"
"v_xor_b32 v164, v121, v18\n"
"v_xor_b32 v165, v123, v18\n"
"v_or_b32 v164, v164, v165\n"
"v_cmp_ne_u32 vcc, 0, v164\n"
"s_cbranch_vccnz LS6\n"
"LB6:\n"
"v_mfma_f32_32x32x16_bf16 v[64:79], a[104:107], a[228:231], v[64:79]\n"
"ds_read_b64_tr_b16 a[202:203], v8 offset:35840\n"
"buffer_load_dwordx4 v[244:247], v16, s[24:27], s73 offen offset:2048 sc1\n"
"ds_read_b64_tr_b16 a[206:207], v9 offset:35840\n"
"v_mfma_f32_32x32x16_bf16 v[64:79], a[112:115], a[232:235], v[64:79]\n"
"ds_read_b64_tr_b16 a[208:209], v8 offset:36864\n"
"ds_read_b64_tr_b16 a[212:213], v9 offset:36864\n"
"ds_read_b64_tr_b16 a[210:211], v8 offset:37888\n"
"v_mfma_f32_32x32x16_bf16 v[96:111], a[116:119], a[232:235], v[96:111]\n"
"ds_read_b64_tr_b16 a[214:215], v9 offset:37888\n"
"buffer_load_dwordx4 v[248:251], v17, s[24:27], s74 offen offset:0 sc1\n"
"s_waitcnt vmcnt(11)\n"
"s_mov_b64 exec, 1\n"
"buffer_store_dword v18, off, s[24:27], s52 sc1\n"
"s_mov_b64 exec, -1\n"
"v_mfma_f32_32x32x16_bf16 v[80:95], a[4:7], a[236:239], 0\n"
"s_mov_b32 m0, s59\n"
"s_nop 0\n"
"buffer_load_dwordx4 v23, s[8:11], s81 offen lds\n"
"ds_read_b64_tr_b16 a[216:217], v8 offset:38912\n"
"v_mfma_f32_32x32x16_bf16 v[48:63], a[0:3], a[236:239], v[48:63]\n"
"buffer_load_dwordx4 v[252:255], v16, s[24:27], s74 offen offset:0 sc1\n"
"ds_read_b64_tr_b16 a[220:221], v9 offset:38912\n"
"buffer_load_dwordx4 v24, s[8:11], s81 offen offset:1024 lds\n"
"s_waitcnt vmcnt(11)\n"
"v_mfma_f32_32x32x16_bf16 v[64:79], a[120:123], a[240:243], v[64:79]\n"
"ds_read_b64_tr_b16 a[218:219], v8 offset:39936\n"
"ds_read_b64_tr_b16 a[222:223], v9 offset:39936\n"
"buffer_load_dwordx4 v[176:179], v17, s[24:27], s74 offen offset:2048 sc1\n"
"v_mfma_f32_32x32x16_bf16 v[96:111], a[124:127], a[240:243], v[96:111]\n"
"v_mfma_f32_32x32x16_bf16 v[80:95], a[12:15], a[244:247], v[80:95]\n"
"v_mfma_f32_32x32x16_bf16 v[48:63], a[8:11], a[244:247], v[48:63]\n"
"buffer_load_dwordx4 v[180:183], v16, s[24:27], s74 offen offset:2048 sc1\n"
"s_waitcnt lgkmcnt(15)\n"
"v_mfma_f32_32x32x16_bf16 v[64:79], a[128:131], a[248:251], v[64:79]\n"
"v_mfma_f32_32x32x16_bf16 v[96:111], a[132:135], a[248:251], v[96:111]\n"
"buffer_load_dwordx4 v[184:187], v17, s[24:27], s75 offen offset:0 sc1\n"
"v_mfma_f32_32x32x16_bf16 v[80:95], a[20:23], a[252:255], v[80:95]\n"
"v_mfma_f32_32x32x16_bf16 v[48:63], a[16:19], a[252:255], v[48:63]\n"
"buffer_load_dwordx4 v[188:191], v16, s[24:27], s75 offen offset:0 sc1\n"
"s_waitcnt vmcnt(11)\n"
"v_mfma_f32_32x32x16_bf16 v[64:79], a[136:139], v[224:227], v[64:79]\n"
"buffer_load_dwordx4 v[192:195], v17, s[24:27], s75 offen offset:2048 sc1\n"
"v_mfma_f32_32x32x16_bf16 v[96:111], a[140:143], v[224:227], v[96:111]\n"
"v_mfma_f32_32x32x16_bf16 v[80:95], a[28:31], v[228:231], v[80:95]\n"
"buffer_load_dwordx4 v[196:199], v16, s[24:27], s75 offen offset:2048 sc1\n"
"v_mfma_f32_32x32x16_bf16 v[48:63], a[24:27], v[228:231], v[48:63]\n"
"v_mfma_f32_32x32x16_bf16 v[64:79], a[144:147], v[232:235], v[64:79]\n"
"buffer_load_dwordx4 v[200:203], v17, s[24:27], s76 offen offset:0 sc1\n"
"v_mfma_f32_32x32x16_bf16 v[96:111], a[148:151], v[232:235], v[96:111]\n"
"v_mfma_f32_32x32x16_bf16 v[80:95], a[36:39], v[236:239], v[80:95]\n"
"buffer_load_dwordx4 v[204:207], v16, s[24:27], s76 offen offset:0 sc1\n"
"v_mfma_f32_32x32x16_bf16 v[48:63], a[32:35], v[236:239], v[48:63]\n"
"s_waitcnt vmcnt(9)\n"
"v_mfma_f32_32x32x16_bf16 v[64:79], a[152:155], v[240:243], v[64:79]\n"
"buffer_load_dwordx4 v[208:211], v17, s[24:27], s76 offen offset:2048 sc1\n"
"v_mfma_f32_32x32x16_bf16 v[96:111], a[156:159], v[240:243], v[96:111]\n"
"v_mfma_f32_32x32x16_bf16 v[80:95], a[44:47], v[244:247], v[80:95]\n"
"buffer_load_dwordx4 v[212:215], v16, s[24:27], s76 offen offset:2048 sc1\n"
"v_mfma_f32_32x32x16_bf16 v[48:63], a[40:43], v[244:247], v[48:63]\n"
"v_mfma_f32_32x32x16_bf16 v[64:79], a[160:163], v[248:251], v[64:79]\n"
"buffer_load_dwordx4 v[216:219], v17, s[24:27], s77 offen offset:0 sc1\n"
"s_waitcnt vmcnt(11)\n"
"ds_read_b64 v[124:125], v26\n"
"v_mfma_f32_32x32x16_bf16 v[96:111], a[164:167], v[248:251], v[96:111]\n"
"ds_read_b64 v[126:127], v27\n"
"buffer_load_dwordx4 v[220:223], v16, s[24:27], s77 offen offset:0 sc1\n"
"ds_read_b64 v[128:129], v28\n"
"v_mfma_f32_32x32x16_bf16 v[80:95], a[52:55], v[252:255], v[80:95]\n"
"ds_read_b64 v[130:131], v29\n"
"v_add_f32 v121, 0x3727c5ac, v122\n"
"v_rsq_f32 v121, v121\n"
"v_mfma_f32_32x32x16_bf16 v[48:63], a[48:51], v[252:255], v[48:63]\n"
"s_waitcnt vmcnt(8)\n"
"v_mfma_f32_32x32x16_bf16 v[64:79], a[168:171], v[176:179], v[64:79]\n"
"buffer_load_dwordx4 a[224:227], v17, s[24:27], s77 offen offset:2048 sc1\n"
"v_mfma_f32_32x32x16_bf16 v[96:111], a[172:175], v[176:179], v[96:111]\n"
"s_waitcnt lgkmcnt(0)\n"
"v_mul_f32 v120, v120, v121\n"
"buffer_load_dwordx4 a[228:231], v16, s[24:27], s77 offen offset:2048 sc1\n"
"v_add_u32 v16, 4096, v16\n"
"v_mfma_f32_32x32x16_bf16 v[80:95], a[60:63], v[180:183], v[80:95]\n"
"v_lshlrev_b32 v148, 16, v124\n"
"v_and_b32 v149, 0xffff0000, v124\n"
"v_fma_f32 v132, v148, v121, -v120\n"
"v_fma_f32 v133, v149, v121, -v120\n"
"v_lshlrev_b32 v150, 16, v125\n"
"v_and_b32 v151, 0xffff0000, v125\n"
"v_mfma_f32_32x32x16_bf16 v[48:63], a[56:59], v[180:183], v[48:63]\n"
"v_fma_f32 v134, v150, v121, -v120\n"
"buffer_load_dwordx4 a[232:235], v16, s[24:27], s71 offen offset:0 sc1\n"
"v_fma_f32 v135, v151, v121, -v120\n"
"v_lshlrev_b32 v152, 16, v126\n"
"v_mfma_f32_32x32x16_bf16 v[64:79], a[176:179], v[184:187], v[64:79]\n"
"v_and_b32 v153, 0xffff0000, v126\n"
"v_fma_f32 v136, v152, v121, -v120\n"
"v_fma_f32 v137, v153, v121, -v120\n"
"v_lshlrev_b32 v154, 16, v127\n"
"v_and_b32 v155, 0xffff0000, v127\n"
"v_mfma_f32_32x32x16_bf16 v[96:111], a[180:183], v[184:187], v[96:111]\n"
"buffer_load_dwordx4 a[236:239], v16, s[24:27], s71 offen offset:2048 sc1\n"
"v_fma_f32 v138, v154, v121, -v120\n"
"v_fma_f32 v139, v155, v121, -v120\n"
"v_lshlrev_b32 v156, 16, v128\n"
"v_and_b32 v157, 0xffff0000, v128\n"
"v_mfma_f32_32x32x16_bf16 v[80:95], a[68:71], v[188:191], v[80:95]\n"
"v_fma_f32 v140, v156, v121, -v120\n"
"v_fma_f32 v141, v157, v121, -v120\n"
"v_lshlrev_b32 v158, 16, v129\n"
"v_and_b32 v159, 0xffff0000, v129\n"
"v_fma_f32 v142, v158, v121, -v120\n"
"v_fma_f32 v143, v159, v121, -v120\n"
"v_mfma_f32_32x32x16_bf16 v[48:63], a[64:67], v[188:191], v[48:63]\n"
"buffer_load_dwordx4 a[240:243], v16, s[24:27], s72 offen offset:0 sc1\n"
"v_lshlrev_b32 v160, 16, v130\n"
"v_and_b32 v161, 0xffff0000, v130\n"
"v_fma_f32 v144, v160, v121, -v120\n"
"v_fma_f32 v145, v161, v121, -v120\n"
"s_waitcnt vmcnt(9)\n"
"v_mfma_f32_32x32x16_bf16 v[64:79], a[184:187], v[192:195], v[64:79]\n"
"v_mov_b32 v174, v18\n"
"v_mov_b32 v175, v18\n"
"s_mov_b32 s50, 17301504\n"
"s_mov_b64 exec, 65535\n"
"buffer_load_dwordx2 v[174:175], v45, s[24:27], s50 offen sc1\n"
"s_mov_b64 exec, -1\n"
"v_mfma_f32_32x32x16_bf16 v[96:111], a[188:191], v[192:195], v[96:111]\n"
"buffer_load_dwordx4 a[244:247], v16, s[24:27], s72 offen offset:2048 sc1\n"
"v_lshlrev_b32 v162, 16, v131\n"
"v_and_b32 v163, 0xffff0000, v131\n"
"v_fma_f32 v146, v162, v121, -v120\n"
"v_fma_f32 v147, v163, v121, -v120\n"
"v_mfma_f32_32x32x16_bf16 v[80:95], a[76:79], v[196:199], v[80:95]\n"
"v_mfma_f32_32x32x16_bf16 v[48:63], a[72:75], v[196:199], v[48:63]\n"
"buffer_load_dwordx4 a[248:251], v16, s[24:27], s73 offen offset:0 sc1\n"
"v_mfma_f32_32x32x16_bf16 v[64:79], a[192:195], v[200:203], v[64:79]\n"
"buffer_load_dwordx4 a[252:255], v16, s[24:27], s73 offen offset:2048 sc1\n"
"v_mfma_f32_32x32x16_bf16 v[96:111], a[196:199], v[200:203], v[96:111]\n"
"v_mfma_f32_32x32x16_bf16 v[80:95], a[84:87], v[204:207], v[80:95]\n"
"buffer_load_dwordx4 v[224:227], v16, s[24:27], s74 offen offset:0 sc1\n"
"v_mfma_f32_32x32x16_bf16 v[48:63], a[80:83], v[204:207], v[48:63]\n"
"s_waitcnt vmcnt(10)\n"
"v_mfma_f32_32x32x16_bf16 v[64:79], a[200:203], v[208:211], v[64:79]\n"
"buffer_load_dwordx4 v[228:231], v16, s[24:27], s74 offen offset:2048 sc1\n"
"v_mfma_f32_32x32x16_bf16 v[96:111], a[204:207], v[208:211], v[96:111]\n"
"v_mfma_f32_32x32x16_bf16 v[80:95], a[92:95], v[212:215], v[80:95]\n"
"buffer_load_dwordx4 v[232:235], v16, s[24:27], s75 offen offset:0 sc1\n"
"v_mfma_f32_32x32x16_bf16 v[48:63], a[88:91], v[212:215], v[48:63]\n"
"v_mfma_f32_32x32x16_bf16 v[64:79], a[208:211], v[216:219], v[64:79]\n"
"buffer_load_dwordx4 v[236:239], v16, s[24:27], s75 offen offset:2048 sc1\n"
"v_mfma_f32_32x32x16_bf16 v[96:111], a[212:215], v[216:219], v[96:111]\n"
"v_mfma_f32_32x32x16_bf16 v[80:95], a[100:103], v[220:223], v[80:95]\n"
"buffer_load_dwordx4 v[240:243], v16, s[24:27], s76 offen offset:0 sc1\n"
"v_mfma_f32_32x32x16_bf16 v[48:63], a[96:99], v[220:223], v[48:63]\n"
"s_waitcnt vmcnt(10)\n"
"v_mfma_f32_32x32x16_bf16 v[64:79], a[216:219], a[224:227], v[64:79]\n"
"buffer_load_dwordx4 v[244:247], v16, s[24:27], s76 offen offset:2048 sc1\n"
"v_mfma_f32_32x32x16_bf16 v[96:111], a[220:223], a[224:227], v[96:111]\n"
"v_mfma_f32_32x32x16_bf16 v[80:95], a[108:111], a[228:231], v[80:95]\n"
"buffer_load_dwordx4 v[248:251], v16, s[24:27], s77 offen offset:0 sc1\n"
"v_mfma_f32_32x32x16_bf16 v[48:63], a[104:107], a[228:231], v[48:63]\n"
"ds_write_b128 v32, v[64:67] offset:0\n"
"v_mfma_f32_32x32x16_bf16 v[48:63], a[112:115], a[232:235], v[48:63]\n"
"buffer_load_dwordx4 v[252:255], v16, s[24:27], s77 offen offset:2048 sc1\n"
"v_mfma_f32_32x32x16_bf16 v[80:95], a[116:119], a[232:235], v[80:95]\n"
"ds_write_b128 v32, v[68:71] offset:1024\n"
"v_mfma_f32_32x32x16_bf16 v[80:95], a[124:127], a[236:239], v[80:95]\n"
"v_mfma_f32_32x32x16_bf16 v[48:63], a[120:123], a[236:239], v[48:63]\n"
"ds_write_b128 v32, v[72:75] offset:2048\n"
"s_waitcnt vmcnt(8)\n"
"v_mfma_f32_32x32x16_bf16 v[48:63], a[128:131], a[240:243], v[48:63]\n"
"v_mfma_f32_32x32x16_bf16 v[80:95], a[132:135], a[240:243], v[80:95]\n"
"ds_write_b128 v32, v[76:79] offset:3072\n"
"v_mfma_f32_32x32x16_bf16 v[80:95], a[140:143], a[244:247], v[80:95]\n"
"v_xor_b32 v174, v174, v18\n"
"v_xor_b32 v175, v175, v18\n"
"v_or_b32 v174, v174, v175\n"
"v_cmp_ne_u32 vcc, 0, v174\n"
"s_cbranch_vccnz LS7\n"
"LB7:\n"
"v_mfma_f32_32x32x16_bf16 v[48:63], a[136:139], a[244:247], v[48:63]\n"
"ds_write_b128 v32, v[96:99] offset:32768\n"
"s_add_u32 s51, s82, 65536\n"
"v_mfma_f32_32x32x16_bf16 v[48:63], a[144:147], a[248:251], v[48:63]\n"
"buffer_load_dwordx4 v[112:115], v25, s[24:27], s82 offen sc1\n"
"v_mfma_f32_32x32x16_bf16 v[80:95], a[148:151], a[248:251], v[80:95]\n"
"ds_write_b128 v32, v[100:103] offset:33792\n"
"v_mfma_f32_32x32x16_bf16 v[80:95], a[156:159], a[252:255], v[80:95]\n"
"buffer_load_dwordx4 v[116:119], v25, s[24:27], s51 offen sc1\n"
"v_mfma_f32_32x32x16_bf16 v[48:63], a[152:155], a[252:255], v[48:63]\n"
"ds_write_b128 v32, v[104:107] offset:34816\n"
"s_waitcnt vmcnt(6)\n"
"v_mfma_f32_32x32x16_bf16 v[48:63], a[160:163], v[224:227], v[48:63]\n"
"v_mfma_f32_32x32x16_bf16 v[80:95], a[164:167], v[224:227], v[80:95]\n"
"ds_write_b128 v32, v[108:111] offset:35840\n"
"v_mfma_f32_32x32x16_bf16 v[80:95], a[172:175], v[228:231], v[80:95]\n"
"v_mfma_f32_32x32x16_bf16 v[48:63], a[168:171], v[228:231], v[48:63]\n"
"v_mfma_f32_32x32x16_bf16 v[48:63], a[176:179], v[232:235], v[48:63]\n"
"s_waitcnt lgkmcnt(0)\n"
"s_barrier\n"
"ds_read_b128 v[64:67], v33 offset:0\n"
"ds_read_b128 v[68:71], v33 offset:1024\n"
"v_mfma_f32_32x32x16_bf16 v[80:95], a[180:183], v[232:235], v[80:95]\n"
"ds_read_b128 v[72:75], v33 offset:2048\n"
"ds_read_b128 v[76:79], v33 offset:3072\n"
"ds_read_b128 v[96:99], v35 offset:32768\n"
"v_mfma_f32_32x32x16_bf16 v[80:95], a[188:191], v[236:239], v[80:95]\n"
"ds_read_b128 v[100:103], v35 offset:33792\n"
"ds_read_b128 v[104:107], v35 offset:34816\n"
"ds_read_b128 v[108:111], v35 offset:35840\n"
"s_waitcnt vmcnt(2)\n"
"v_mfma_f32_32x32x16_bf16 v[80:95], a[196:199], v[240:243], v[80:95]\n"
"v_mfma_f32_32x32x16_bf16 v[80:95], a[204:207], v[244:247], v[80:95]\n"
"v_mfma_f32_32x32x16_bf16 v[80:95], a[212:215], v[248:251], v[80:95]\n"
"v_mfma_f32_32x32x16_bf16 v[80:95], a[220:223], v[252:255], v[80:95]\n"
"s_waitcnt lgkmcnt(0)\n"
"v_add_f32 v64, v64, v96\n"
"v_add_f32 v65, v65, v97\n"
"v_add_f32 v66, v66, v98\n"
"v_add_f32 v67, v67, v99\n"
"v_add_f32 v68, v68, v100\n"
"v_mfma_f32_32x32x16_bf16 v[48:63], a[184:187], v[236:239], v[48:63]\n"
"v_add_f32 v69, v69, v101\n"
"v_add_f32 v70, v70, v102\n"
"v_add_f32 v71, v71, v103\n"
"v_add_f32 v72, v72, v104\n"
"v_add_f32 v73, v73, v105\n"
"v_add_f32 v74, v74, v106\n"
"v_mfma_f32_32x32x16_bf16 v[48:63], a[192:195], v[240:243], v[48:63]\n"
"ds_write_b128 v32, v[80:83] offset:4096\n"
"v_mfma_f32_32x32x16_bf16 v[48:63], a[200:203], v[244:247], v[48:63]\n"
"v_add_f32 v75, v75, v107\n"
"v_add_f32 v76, v76, v108\n"
"v_add_f32 v77, v77, v109\n"
"v_add_f32 v78, v78, v110\n"
"v_add_f32 v79, v79, v111\n"
"v_mfma_f32_32x32x16_bf16 v[48:63], a[208:211], v[248:251], v[48:63]\n"
"ds_write_b128 v32, v[84:87] offset:5120\n"
"v_mfma_f32_32x32x16_bf16 v[48:63], a[216:219], v[252:255], v[48:63]\n"
"ds_write_b128 v32, v[88:91] offset:6144\n"
"ds_write_b128 v32, v[92:95] offset:7168\n"
"s_waitcnt lgkmcnt(0)\n"
"s_barrier\n"
"ds_read_b128 v[80:83], v34 offset:4096\n"
"ds_read_b128 v[84:87], v34 offset:5120\n"
"ds_read_b128 v[88:91], v34 offset:6144\n"
"ds_read_b128 v[92:95], v34 offset:7168\n"
"s_waitcnt vmcnt(0)\n"
"ds_write_b128 v30, v[112:115] offset:2048\n"
"ds_write_b128 v30, v[116:119] offset:3072\n"
"s_nop 0\n"
"v_pk_add_f32 v[48:49], v[48:49], v[64:65]\n"
"v_pk_add_f32 v[50:51], v[50:51], v[66:67]\n"
"v_pk_add_f32 v[52:53], v[52:53], v[68:69]\n"
"v_pk_add_f32 v[54:55], v[54:55], v[70:71]\n"
"v_pk_add_f32 v[56:57], v[56:57], v[72:73]\n"
"v_pk_add_f32 v[58:59], v[58:59], v[74:75]\n"
"v_pk_add_f32 v[60:61], v[60:61], v[76:77]\n"
"v_pk_add_f32 v[62:63], v[62:63], v[78:79]\n"
"s_waitcnt lgkmcnt(0)\n"
"ds_read_b64_tr_b16 v[166:167], v31 offset:2048\n"
"ds_read_b64_tr_b16 v[168:169], v31 offset:2560\n"
"ds_read_b64_tr_b16 v[170:171], v31 offset:3072\n"
"ds_read_b64_tr_b16 v[172:173], v31 offset:3584\n"
"v_pk_add_f32 v[48:49], v[48:49], v[80:81]\n"
"v_pk_add_f32 v[50:51], v[50:51], v[82:83]\n"
"v_pk_add_f32 v[52:53], v[52:53], v[84:85]\n"
"v_pk_add_f32 v[54:55], v[54:55], v[86:87]\n"
"v_pk_add_f32 v[56:57], v[56:57], v[88:89]\n"
"v_pk_add_f32 v[58:59], v[58:59], v[90:91]\n"
"v_pk_add_f32 v[60:61], v[60:61], v[92:93]\n"
"v_pk_add_f32 v[62:63], v[62:63], v[94:95]\n"
"s_waitcnt lgkmcnt(0)\n"
"v_lshlrev_b32 v116, 16, v166\n"
"v_and_b32 v117, 0xffff0000, v166\n"
"v_pk_fma_f32 v[148:149], v[132:133], v[116:117], v[148:149]\n"
"v_lshlrev_b32 v118, 16, v167\n"
"v_and_b32 v119, 0xffff0000, v167\n"
"v_pk_fma_f32 v[150:151], v[134:135], v[118:119], v[150:151]\n"
"v_lshlrev_b32 v112, 16, v168\n"
"v_and_b32 v113, 0xffff0000, v168\n"
"v_pk_fma_f32 v[152:153], v[136:137], v[112:113], v[152:153]\n"
"v_lshlrev_b32 v114, 16, v169\n"
"v_and_b32 v115, 0xffff0000, v169\n"
"v_pk_fma_f32 v[154:155], v[138:139], v[114:115], v[154:155]\n"
"v_lshlrev_b32 v116, 16, v170\n"
"v_and_b32 v117, 0xffff0000, v170\n"
"v_pk_fma_f32 v[156:157], v[140:141], v[116:117], v[156:157]\n"
"v_lshlrev_b32 v118, 16, v171\n"
"v_and_b32 v119, 0xffff0000, v171\n"
"v_pk_fma_f32 v[158:159], v[142:143], v[118:119], v[158:159]\n"
"v_lshlrev_b32 v112, 16, v172\n"
"v_and_b32 v113, 0xffff0000, v172\n"
"v_pk_fma_f32 v[160:161], v[144:145], v[112:113], v[160:161]\n"
"v_lshlrev_b32 v114, 16, v173\n"
"v_and_b32 v115, 0xffff0000, v173\n"
"v_pk_fma_f32 v[162:163], v[146:147], v[114:115], v[162:163]\n"
"v_pk_add_f32 v[48:49], v[48:49], v[148:149]\n"
"v_pk_add_f32 v[50:51], v[50:51], v[150:151]\n"
"v_pk_add_f32 v[52:53], v[52:53], v[152:153]\n"
"v_pk_add_f32 v[54:55], v[54:55], v[154:155]\n"
"v_pk_add_f32 v[56:57], v[56:57], v[156:157]\n"
"v_pk_add_f32 v[58:59], v[58:59], v[158:159]\n"
"v_pk_add_f32 v[60:61], v[60:61], v[160:161]\n"
"v_pk_add_f32 v[62:63], v[62:63], v[162:163]\n"
"v_cvt_pk_bf16_f32 v112, v48, v49\n"
"v_cvt_pk_bf16_f32 v113, v50, v51\n"
"ds_write_b64 v26, v[112:113]\n"
"v_cvt_pk_bf16_f32 v114, v52, v53\n"
"v_cvt_pk_bf16_f32 v115, v54, v55\n"
"ds_write_b64 v27, v[114:115]\n"
"v_cvt_pk_bf16_f32 v116, v56, v57\n"
"v_cvt_pk_bf16_f32 v117, v58, v59\n"
"ds_write_b64 v28, v[116:117]\n"
"v_cvt_pk_bf16_f32 v118, v60, v61\n"
"v_cvt_pk_bf16_f32 v119, v62, v63\n"
"ds_write_b64 v29, v[118:119]\n"
"s_waitcnt lgkmcnt(0)\n"
"ds_read_b128 v[120:123], v30\n"
"ds_read_b128 v[124:127], v30 offset:1024\n"
"s_waitcnt lgkmcnt(1)\n"
"buffer_store_dwordx4 v[120:123], v23, s[36:39], s81 offen sc1\n"
"s_waitcnt lgkmcnt(0)\n"
"buffer_store_dwordx4 v[124:127], v24, s[36:39], s81 offen offset:1024 sc1\n"
"s_endpgm\n"
"LHELP:\n"
"s_mov_b32 m0, s53\n"
"global_load_dwordx2 v[162:163], v221, s[40:41]\n"
"s_mov_b32 exec_lo, 0\n"
"buffer_load_dwordx4 v215, s[16:19], 0 offen lds\n"
"s_mov_b32 exec_lo, -1\n"
"s_add_u32 m0, s53, 1024\n"
"s_nop 0\n"
"buffer_load_dwordx4 v2, s[16:19], s52 offen lds\n"
"buffer_load_dwordx4 v3, s[16:19], s52 offen offset:1024 lds\n"
"s_add_u32 m0, s53, 3072\n"
"s_nop 0\n"
"buffer_load_dwordx4 v2, s[20:23], s52 offen lds\n"
"buffer_load_dwordx4 v3, s[20:23], s52 offen offset:1024 lds\n"
"s_lshr_b32 s58, s6, 1\n"
"s_lshl_b32 s58, s58, 2\n"
"s_add_u32 s58, s58, s7\n"
"s_sub_u32 s58, s58, 28\n"
"s_min_u32 s49, s58, 13\n"
"s_and_b32 s50, s6, 1\n"
"s_mul_i32 s50, s50, 14\n"
"s_add_u32 s49, s49, s50\n"
"s_lshl_b32 s60, s49, 7\n"
"s_mul_i32 s50, s5, 114688\n"
"s_add_u32 s60, s60, s50\n"
"s_add_u32 s60, s60, 0\n"
"s_lshl_b32 s49, s49, 9\n"
"s_add_u32 s49, s49, 16842752\n"
"s_cmp_lt_u32 s58, 14\n"
"s_cselect_b32 s49, s49, 17567744\n"
"s_cselect_b32 s50, 0, 0x40000000\n"
"s_cmp_eq_u32 s5, 0\n"
"s_cselect_b32 s58, s49, 17568768\n"
"v_mul_u32_u24 v240, 3584, v227\n"
"v_and_b32 v241, 0x70, v2\n"
"v_add3_u32 v240, v240, v241, s50\n"
"v_add_u32 v241, 27648, v240\n"
"v_add_u32 v242, 55296, v240\n"
"v_add_u32 v243, 82944, v240\n"
"s_lshr_b32 s50, s6, 1\n"
"s_add_u32 m0, s78, 32768\n"
"s_nop 0\n"
"buffer_load_dwordx4 v240, s[12:15], s60 offen offset:0 lds\n"
"s_lshl_b32 s50, s50, 2\n"
"s_and_b32 s56, s7, 3\n"
"s_add_u32 s56, s56, s50\n"
"buffer_load_dwordx4 v241, s[12:15], s60 offen offset:1024 lds\n"
"s_and_b32 s50, s6, 1\n"
"s_mul_i32 s50, s50, 7\n"
"s_lshr_b32 s57, s7, 2\n"
"buffer_load_dwordx4 v242, s[12:15], s60 offen offset:2048 lds\n"
"s_add_u32 s66, s7, s57\n"
"s_and_b32 s66, s66, 3\n"
"s_mov_b32 s67, 0\n"
"buffer_load_dwordx4 v243, s[12:15], s60 offen offset:3072 lds\n"
"s_lshl_b32 s67, s67, 7\n"
"s_add_u32 s57, s57, s50\n"
"s_lshl_b32 s50, s56, 17\n"
"s_add_u32 m0, s78, 36864\n"
"s_add_u32 s60, s60, 458752\n"
"buffer_load_dwordx4 v240, s[12:15], s60 offen offset:0 lds\n"
"s_lshl_b32 s66, s5, 15\n"
"s_add_u32 s50, s50, s66\n"
"v_add_u32 v224, s50, v223\n"
"buffer_load_dwordx4 v241, s[12:15], s60 offen offset:1024 lds\n"
"v_xor_b32 v222, v228, v229\n"
"v_lshl_add_u32 v236, v222, 4, v224\n"
"v_xor_b32 v222, 4, v222\n"
"buffer_load_dwordx4 v242, s[12:15], s60 offen offset:2048 lds\n"
"v_lshl_add_u32 v237, v222, 4, v224\n"
"v_add_u32 v238, 14336, v236\n"
"v_add_u32 v239, 21504, v237\n"
"buffer_load_dwordx4 v243, s[12:15], s60 offen offset:3072 lds\n"
"v_add_u32 v237, 7168, v237\n"
"s_mul_i32 s50, s5, 57344\n"
"s_lshl_b32 s66, s57, 8\n"
"s_add_u32 m0, s78, 65536\n"
"s_add_u32 s60, s60, 458752\n"
"buffer_load_dwordx4 v240, s[12:15], s60 offen offset:0 lds\n"
"s_add_u32 s50, s50, s66\n"
"v_mul_u32_u24 v224, 3584, v229\n"
"v_and_b32 v225, 15, v0\n"
"v_lshlrev_b32 v222, 2, v229\n"
"v_xor_b32 v222, v225, v222\n"
"v_lshl_add_u32 v224, v222, 4, v224\n"
"s_mov_b32 s50, 17563648\n"
"v_lshlrev_b32 v226, 5, v233\n"
"v_lshl_add_u32 v226, v231, 3, v226\n"
"v_lshrrev_b32 v222, 1, v230\n"
"v_xor_b32 v224, v232, v222\n"
"v_lshl_add_u32 v224, v224, 6, v226\n"
"v_lshl_add_u32 v224, v230, 7, v224\n"
"v_add_u32 v209, s53, v224\n"
"s_lshr_b32 s66, s5, 1\n"
"v_xor_b32 v222, s66, v222\n"
"v_lshl_add_u32 v224, v232, 2, v230\n"
"v_lshl_add_u32 v31, v224, 6, v226\n"
"v_lshl_add_u32 v224, v224, 7, v226\n"
"v_lshl_add_u32 v10, v222, 6, v224\n"
"v_lshl_add_u32 v224, v232, 3, v230\n"
"v_lshl_add_u32 v224, v224, 8, v226\n"
"s_and_b32 s66, s5, 2\n"
"v_xor_b32 v222, s66, v230\n"
"v_lshl_add_u32 v244, v222, 6, v224\n"
"v_add_u32 v244, 16384, v244\n"
"v_xor_b32 v245, 64, v244\n"
"v_add_u32 v246, 65536, v244\n"
"v_add_u32 v247, 65536, v245\n"
"s_and_b32 s61, s5, 1\n"
"s_lshl_b32 s66, s61, 13\n"
"v_lshlrev_b32 v224, 7, v234\n"
"v_add_u32 v224, s66, v224\n"
"v_bfe_u32 v223, v0, 1, 3\n"
"v_xor_b32 v222, v223, v232\n"
"v_lshl_add_u32 v248, v222, 4, v224\n"
"v_xor_b32 v249, 32, v248\n"
"v_xor_b32 v250, 64, v248\n"
"v_xor_b32 v251, 96, v248\n"
"v_add_u32 v252, 65536, v248\n"
"v_add_u32 v253, 65536, v249\n"
"v_add_u32 v254, 65536, v250\n"
"v_add_u32 v255, 65536, v251\n"
"s_lshl_b32 s62, s5, 1\n"
"s_sub_u32 s62, 6, s62\n"
"v_xor_b32 v222, s62, v228\n"
"v_lshlrev_b32 v224, 7, v0\n"
"v_lshl_add_u32 v210, v222, 4, v224\n"
"v_add_u32 v210, 151552, v210\n"
"v_xor_b32 v211, 16, v210\n"
"v_xor_b32 v222, v228, v227\n"
"v_lshlrev_b32 v222, 4, v222\n"
"v_lshl_add_u32 v222, v227, 7, v222\n"
"s_lshl_b32 s62, s5, 11\n"
"s_add_u32 s62, s62, 151552\n"
"v_add_u32 v212, s62, v222\n"
"v_lshlrev_b32 v222, 4, v228\n"
"v_lshl_add_u32 v214, v227, 12, v222\n"
"s_lshl_b32 s79, s6, 18\n"
"s_lshl_b32 s62, s5, 16\n"
"s_add_u32 s79, s79, s62\n"
"s_lshl_b32 s62, s7, 7\n"
"s_lshr_b32 s54, s79, 4\n"
"s_add_u32 s54, s54, s62\n"
"s_sub_u32 s79, s79, s62\n"
"s_add_u32 s79, s79, 8392576\n"
"v_add_u32 v6, s78, v10\n"
"v_xor_b32 v7, 64, v6\n"
"v_add_u32 v8, 65536, v6\n"
"v_add_u32 v9, 65536, v7\n"
"s_lshl_b32 s62, s61, 10\n"
"v_add_u32 v16, s62, v1\n"
"v_xor_b32 v17, 1024, v16\n"
"v_lshlrev_b32 v42, 3, v234\n"
"s_add_u32 s62, s78, 32768\n"
"v_add_u32 v32, s62, v1\n"
"v_xor_b32 v33, 8192, v32\n"
"v_xor_b32 v34, 16384, v32\n"
"v_xor_b32 v35, 24576, v32\n"
"s_mov_b32 s62, s59\n"
"v_add_u32 v30, s62, v1\n"
"v_add_u32 v31, s62, v31\n"
"v_lshlrev_b32 v222, 6, v234\n"
"v_lshl_add_u32 v222, v230, 4, v222\n"
"v_lshl_add_u32 v222, v232, 3, v222\n"
"v_add_u32 v26, s62, v222\n"
"v_xor_b32 v27, 16, v26\n"
"v_xor_b32 v28, 32, v26\n"
"v_xor_b32 v29, 48, v26\n"
"v_lshrrev_b32 v222, 2, v0\n"
"v_lshlrev_b32 v224, 4, v231\n"
"v_lshl_add_u32 v25, v222, 12, v224\n"
"v_lshlrev_b32 v222, 10, v222\n"
"v_xor_b32 v224, v231, v229\n"
"v_lshl_add_u32 v23, v224, 4, v222\n"
"v_add_u32 v24, 15360, v23\n"
"s_lshr_b32 s51, s5, 1\n"
"s_bfe_u32 s50, s4, 0x20005\n"
"s_cmp_eq_u32 s50, 3\n"
"s_cselect_b32 s50, 128, 0\n"
"s_xor_b32 s49, s4, s50\n"
"s_lshl_b32 s49, s49, 1\n"
"s_add_u32 s49, s49, s51\n"
"s_lshl_b32 s50, s49, 1\n"
"v_mov_b32 v22, s50\n"
"s_lshl_b32 s49, s49, 12\n"
"s_lshl_b32 s63, s61, 11\n"
"s_add_u32 s100, s49, s63\n"
"s_add_u32 s3, s100, 8388608\n"
"s_add_u32 s80, s100, 12582912\n"
"v_add_u32 v39, s63, v1\n"
"s_mul_i32 s49, s51, 4224\n"
"s_add_u32 s49, s49, 131072\n"
"s_add_u32 s101, s49, s63\n"
"s_add_u32 s101, s101, 128\n"
"s_xor_b32 s50, s51, 1\n"
"s_mul_i32 s50, s50, 139520\n"
"v_add_u32 v39, s50, v39\n"
"v_and_b32 v222, 0x70, v1\n"
"v_add_u32 v222, s49, v222\n"
"s_mul_i32 s62, s61, 9024\n"
"s_add_u32 s62, s62, s50\n"
"s_add_u32 s62, s62, 4096\n"
"v_add_u32 v40, s62, v1\n"
"v_cmp_lt_u32 vcc, 23, v0\n"
"v_add_u32 v224, 4128, v40\n"
"s_lshr_b32 s62, s63, 3\n"
"v_cndmask_b32 v40, v40, v224, vcc\n"
"v_cmp_lt_u32 vcc, 47, v0\n"
"s_add_u32 s49, s49, s62\n"
"s_add_u32 s49, s49, 8\n"
"v_cndmask_b32 v40, v40, v222, vcc\n"
"v_lshlrev_b32 v222, 3, v225\n"
"v_lshl_add_u32 v222, v229, 4, v222\n"
"v_add_u32 v13, s49, v222\n"
"v_sub_u32 v222, 3, v231\n"
"v_mul_u32_u24 v222, 4512, v222\n"
"v_lshlrev_b32 v224, 7, v230\n"
"v_sub_u32 v222, v222, v224\n"
"v_lshl_add_u32 v222, v229, 4, v222\n"
"s_add_u32 s62, s62, s50\n"
"s_add_u32 s62, s62, 384\n"
"v_add_u32 v12, s62, v222\n"
"s_add_u32 s50, s50, 2552\n"
"s_sub_u32 s50, s50, s63\n"
"v_sub_u32 v14, s50, v44\n"
"s_add_u32 s0, s59, 18432\n"
"s_cmp_eq_u32 s5, 3\n"
"s_cselect_b32 s0, 157696, s0\n"
"v_add_u32 v10, s0, v10\n"
"v_xor_b32 v11, 64, v10\n"
"v_add_u32 v19, s0, v1\n"
"s_xor_b32 s49, s59, 4096\n"
"s_add_u32 s49, s49, 18432\n"
"s_cmp_eq_u32 s5, 2\n"
"s_cselect_b32 s49, 157696, s49\n"
"v_add_u32 v20, s49, v1\n"
"s_lshr_b32 s70, s6, 1\n"
"s_lshl_b32 s70, s70, 3\n"
"s_and_b32 s49, s7, 7\n"
"s_add_u32 s70, s70, s49\n"
"s_and_b32 s77, s6, 1\n"
"s_mul_i32 s77, s77, 5\n"
"s_lshr_b32 s49, s7, 3\n"
"s_add_u32 s77, s77, s49\n"
"s_lshl_b32 s50, s6, 2\n"
"s_and_b32 s63, s7, 3\n"
"s_add_u32 s50, s50, s63\n"
"s_lshr_b32 s63, s7, 2\n"
"s_sub_u32 s63, 10, s63\n"
"s_cmp_eq_u32 s49, 3\n"
"s_cselect_b32 s70, s50, s70\n"
"s_cmp_eq_u32 s49, 3\n"
"s_cselect_b32 s77, s63, s77\n"
"s_lshl_b32 s1, s70, 13\n"
"s_mul_i32 s49, s5, 896\n"
"s_add_u32 s1, s1, s49\n"
"s_add_u32 s1, s1, 17039360\n"
"s_add_u32 s2, s1, 4096\n"
"s_lshl_b32 s49, s70, 1\n"
"s_add_u32 s49, s49, s61\n"
"s_lshl_b32 s50, s77, 1\n"
"s_add_u32 s50, s50, s51\n"
"s_lshl_b32 s81, s49, 15\n"
"s_lshl_b32 s63, s50, 6\n"
"v_lshl_add_u32 v36, v234, 1, s63\n"
"s_add_u32 s81, s81, s63\n"
"s_lshl_b32 s63, s50, 11\n"
"v_add_u32 v45, s63, v15\n"
"s_lshr_b32 s63, s70, 4\n"
"s_lshl_b32 s63, s63, 15\n"
"v_add_u32 v45, s63, v45\n"
"s_lshl_b32 s82, s50, 17\n"
"s_lshl_b32 s63, s49, 6\n"
"s_add_u32 s82, s82, s63\n"
"s_add_u32 s82, s82, 12582912\n"
"s_lshl_b32 s63, s49, 9\n"
"v_add_u32 v46, s63, v37\n"
"s_mul_i32 s49, s5, 458752\n"
"s_lshl_b32 s50, s77, 7\n"
"s_add_u32 s49, s49, s50\n"
"s_mul_i32 s50, s70, 229376\n"
"s_mul_i32 s51, s5, 57344\n"
"s_add_u32 s50, s50, s51\n"
"s_add_u32 s51, s70, s77\n"
"s_and_b32 s51, s51, 3\n"
"s_mov_b32 s62, 0x06534210\n"
"s_cmp_eq_u32 s51, 1\n"
"s_cselect_b32 s62, 0x06453201, s62\n"
"s_cmp_eq_u32 s51, 2\n"
"s_cselect_b32 s62, 0x05643120, s62\n"
"s_cmp_eq_u32 s51, 3\n"
"s_cselect_b32 s62, 0x06542310, s62\n"
"s_min_u32 s76, s6, 6\n"
"s_cmp_eq_u32 s6, 7\n"
"s_cselect_b32 s76, 3, s76\n"
"s_bfe_u32 s51, s62, 0x30000\n"
"s_add_u32 s51, s51, s76\n"
"s_sub_u32 s63, s51, 7\n"
"s_min_u32 s51, s51, s63\n"
"s_lshl_b32 s63, s51, 16\n"
"s_add_u32 s64, s49, s63\n"
"s_lshl_b32 s63, s51, 13\n"
"s_add_u32 s71, s50, s63\n"
"s_bfe_u32 s51, s62, 0x30004\n"
"s_add_u32 s51, s51, s76\n"
"s_sub_u32 s63, s51, 7\n"
"s_min_u32 s51, s51, s63\n"
"s_lshl_b32 s63, s51, 16\n"
"s_add_u32 s65, s49, s63\n"
"s_lshl_b32 s63, s51, 13\n"
"s_add_u32 s72, s50, s63\n"
"s_bfe_u32 s51, s62, 0x30008\n"
"s_add_u32 s51, s51, s76\n"
"s_sub_u32 s63, s51, 7\n"
"s_min_u32 s51, s51, s63\n"
"s_lshl_b32 s63, s51, 16\n"
"s_add_u32 s66, s49, s63\n"
"s_lshl_b32 s63, s51, 13\n"
"s_add_u32 s73, s50, s63\n"
"s_bfe_u32 s51, s62, 0x3000c\n"
"s_add_u32 s51, s51, s76\n"
"s_sub_u32 s63, s51, 7\n"
"s_min_u32 s51, s51, s63\n"
"s_lshl_b32 s63, s51, 16\n"
"s_add_u32 s67, s49, s63\n"
"s_lshl_b32 s63, s51, 13\n"
"s_add_u32 s74, s50, s63\n"
"s_bfe_u32 s51, s62, 0x30010\n"
"s_add_u32 s51, s51, s76\n"
"s_sub_u32 s63, s51, 7\n"
"s_min_u32 s51, s51, s63\n"
"s_lshl_b32 s63, s51, 16\n"
"s_add_u32 s68, s49, s63\n"
"s_lshl_b32 s63, s51, 13\n"
"s_add_u32 s75, s50, s63\n"
"s_bfe_u32 s51, s62, 0x30014\n"
"s_add_u32 s51, s51, s76\n"
"s_mov_b32 s77, s76\n"
"s_sub_u32 s63, s51, 7\n"
"s_min_u32 s51, s51, s63\n"
"s_lshl_b32 s63, s51, 16\n"
"s_add_u32 s69, s49, s63\n"
"s_lshl_b32 s63, s51, 13\n"
"s_add_u32 s76, s50, s63\n"
"s_bfe_u32 s51, s62, 0x30018\n"
"s_add_u32 s51, s51, s77\n"
"s_sub_u32 s63, s51, 7\n"
"s_min_u32 s51, s51, s63\n"
"s_lshl_b32 s63, s51, 16\n"
"s_add_u32 s70, s49, s63\n"
"s_lshl_b32 s63, s51, 13\n"
"s_add_u32 s77, s50, s63\n"
"v_mov_b32 v89, 0x3f803f80\n"
"s_waitcnt vmcnt(15)\n"
"v_readfirstlane_b32 s48, v66\n"
"v_dot2_f32_bf16 v216, v76, v89, 0\n"
"buffer_load_dwordx4 v241, s[12:15], s60 offen offset:1024 lds\n"
"v_dot2_f32_bf16 v218, v76, v76, 0\n"
"v_dot2_f32_bf16 v216, v77, v89, v216\n"
"v_dot2_f32_bf16 v218, v77, v77, v218\n"
"v_dot2_f32_bf16 v216, v78, v89, v216\n"
"v_dot2_f32_bf16 v218, v78, v78, v218\n"
"v_dot2_f32_bf16 v216, v79, v89, v216\n"
"buffer_load_dwordx4 v242, s[12:15], s60 offen offset:2048 lds\n"
"v_dot2_f32_bf16 v218, v79, v79, v218\n"
"v_dot2_f32_bf16 v216, v80, v89, v216\n"
"v_dot2_f32_bf16 v218, v80, v80, v218\n"
"v_dot2_f32_bf16 v216, v81, v89, v216\n"
"v_dot2_f32_bf16 v218, v81, v81, v218\n"
"v_dot2_f32_bf16 v216, v82, v89, v216\n"
"buffer_load_dwordx4 v243, s[12:15], s60 offen offset:3072 lds\n"
"v_dot2_f32_bf16 v218, v82, v82, v218\n"
"v_dot2_f32_bf16 v216, v83, v89, v216\n"
"v_dot2_f32_bf16 v218, v83, v83, v218\n"
"s_mul_i32 s48, s48, 1664525\n"
"s_add_u32 s48, s48, 1013904223\n"
"v_add_f32_dpp v216, v216, v216 row_shr:1 row_mask:0xf bank_mask:0xf bound_ctrl:1\n"
"s_add_u32 m0, s78, 69632\n"
"s_add_u32 s60, s60, 458752\n"
"buffer_load_dwordx4 v240, s[12:15], s60 offen offset:0 lds\n"
"v_add_f32_dpp v218, v218, v218 row_shr:1 row_mask:0xf bank_mask:0xf bound_ctrl:1\n"
"v_mov_b32 v18, s48\n"
"v_add_f32_dpp v216, v216, v216 row_shr:2 row_mask:0xf bank_mask:0xf bound_ctrl:1\n"
"v_add_f32_dpp v218, v218, v218 row_shr:2 row_mask:0xf bank_mask:0xf bound_ctrl:1\n"
"v_mov_b32 v217, s48\n"
"v_add_f32_dpp v216, v216, v216 row_shr:4 row_mask:0xf bank_mask:0xf bound_ctrl:1\n"
"buffer_load_dwordx4 v241, s[12:15], s60 offen offset:1024 lds\n"
"v_add_f32_dpp v218, v218, v218 row_shr:4 row_mask:0xf bank_mask:0xf bound_ctrl:1\n"
"v_mov_b32 v219, s48\n"
"v_add_f32_dpp v216, v216, v216 row_shr:8 row_mask:0xf bank_mask:0xf bound_ctrl:1\n"
"v_add_f32_dpp v218, v218, v218 row_shr:8 row_mask:0xf bank_mask:0xf bound_ctrl:1\n"
"v_lshlrev_b32 v220, 4, v232\n"
"v_add_f32_dpp v216, v216, v216 row_bcast:15 row_mask:0xa bank_mask:0xf\n"
"buffer_load_dwordx4 v242, s[12:15], s60 offen offset:2048 lds\n"
"v_add_f32_dpp v218, v218, v218 row_bcast:15 row_mask:0xa bank_mask:0xf\n"
"v_mul_f32 v216, 0x3b000000, v216\n"
"v_mul_f32 v218, 0x3b000000, v218\n"
"v_fma_f32 v218, -v216, v216, v218\n"
"v_max_f32 v218, 0, v218\n"
"s_waitcnt vmcnt(17)\n"
"buffer_load_dwordx4 v243, s[12:15], s60 offen offset:3072 lds\n"
"ds_read_b64_tr_b16 v[144:145], v209 offset:512\n"
"ds_read_b64_tr_b16 v[146:147], v209 offset:1024\n"
"ds_read_b64_tr_b16 v[148:149], v209 offset:1536\n"
"ds_read_b64_tr_b16 v[150:151], v209 offset:2048\n"
"ds_read_b64_tr_b16 v[152:153], v209 offset:2560\n"
"v_mov_b32 v207, 0xffff\n"
"v_alignbit_b32 v204, v162, v162, 16\n"
"v_alignbit_b32 v205, v163, v163, 16\n"
"v_and_b32 v206, 0xffff, v162\n"
"v_bfi_b32 v207, v207, v163, v162\n"
"v_and_b32 v208, 0xffff0000, v163\n"
"s_waitcnt lgkmcnt(0)\n"
"v_dot2_f32_bf16 v172, v144, v208, 0\n"
"v_dot2_f32_bf16 v173, v145, v205, 0\n"
"v_dot2_f32_bf16 v174, v145, v208, 0\n"
"v_dot2_f32_bf16 v175, v146, v205, 0\n"
"v_dot2_f32_bf16 v176, v146, v208, 0\n"
"v_dot2_f32_bf16 v177, v147, v205, 0\n"
"v_dot2_f32_bf16 v178, v147, v208, 0\n"
"v_dot2_f32_bf16 v179, v148, v205, 0\n"
"v_dot2_f32_bf16 v180, v148, v208, 0\n"
"v_dot2_f32_bf16 v181, v149, v205, 0\n"
"v_dot2_f32_bf16 v182, v149, v208, 0\n"
"v_dot2_f32_bf16 v183, v150, v205, 0\n"
"v_dot2_f32_bf16 v184, v150, v208, 0\n"
"v_dot2_f32_bf16 v185, v151, v205, 0\n"
"v_dot2_f32_bf16 v186, v151, v208, 0\n"
"v_dot2_f32_bf16 v187, v152, v205, 0\n"
"v_dot2_f32_bf16 v172, v145, v207, v172\n"
"v_dot2_f32_bf16 v173, v146, v204, v173\n"
"v_dot2_f32_bf16 v174, v146, v207, v174\n"
"v_dot2_f32_bf16 v175, v147, v204, v175\n"
"s_waitcnt vmcnt(16)\n"
"ds_read_b64_tr_b16 v[154:155], v209 offset:3072\n"
"ds_read_b64_tr_b16 v[156:157], v209 offset:3584\n"
"ds_read_b64_tr_b16 v[158:159], v209 offset:4096\n"
"ds_read_b64_tr_b16 v[160:161], v209 offset:4608\n"
"v_dot2_f32_bf16 v176, v147, v207, v176\n"
"v_dot2_f32_bf16 v177, v148, v204, v177\n"
"v_dot2_f32_bf16 v178, v148, v207, v178\n"
"v_dot2_f32_bf16 v179, v149, v204, v179\n"
"v_dot2_f32_bf16 v180, v149, v207, v180\n"
"v_dot2_f32_bf16 v181, v150, v204, v181\n"
"v_dot2_f32_bf16 v182, v150, v207, v182\n"
"v_dot2_f32_bf16 v183, v151, v204, v183\n"
"v_dot2_f32_bf16 v184, v151, v207, v184\n"
"v_dot2_f32_bf16 v185, v152, v204, v185\n"
"v_dot2_f32_bf16 v186, v152, v207, v186\n"
"v_dot2_f32_bf16 v187, v153, v204, v187\n"
"v_dot2_f32_bf16 v172, v146, v206, v172\n"
"v_dot2_f32_bf16 v174, v147, v206, v174\n"
"v_dot2_f32_bf16 v176, v148, v206, v176\n"
"v_dot2_f32_bf16 v178, v149, v206, v178\n"
"v_dot2_f32_bf16 v180, v150, v206, v180\n"
"v_dot2_f32_bf16 v182, v151, v206, v182\n"
"v_dot2_f32_bf16 v184, v152, v206, v184\n"
"v_dot2_f32_bf16 v186, v153, v206, v186\n"
"s_waitcnt lgkmcnt(0)\n"
"v_lshlrev_b32 v188, 16, v154\n"
"v_and_b32 v189, 0xffff0000, v154\n"
"v_lshlrev_b32 v190, 16, v155\n"
"v_and_b32 v191, 0xffff0000, v155\n"
"v_lshlrev_b32 v192, 16, v156\n"
"v_and_b32 v193, 0xffff0000, v156\n"
"v_lshlrev_b32 v194, 16, v157\n"
"v_and_b32 v195, 0xffff0000, v157\n"
"v_lshlrev_b32 v196, 16, v158\n"
"v_and_b32 v197, 0xffff0000, v158\n"
"v_lshlrev_b32 v198, 16, v159\n"
"v_and_b32 v199, 0xffff0000, v159\n"
"v_lshlrev_b32 v200, 16, v160\n"
"v_and_b32 v201, 0xffff0000, v160\n"
"v_lshlrev_b32 v202, 16, v161\n"
"v_and_b32 v203, 0xffff0000, v161\n"
"v_pk_mul_f32 v[172:173], v[172:173], v[188:189]\n"
"v_pk_mul_f32 v[174:175], v[174:175], v[190:191]\n"
"v_pk_mul_f32 v[176:177], v[176:177], v[192:193]\n"
"v_pk_mul_f32 v[178:179], v[178:179], v[194:195]\n"
"v_pk_mul_f32 v[180:181], v[180:181], v[196:197]\n"
"v_pk_mul_f32 v[182:183], v[182:183], v[198:199]\n"
"v_pk_mul_f32 v[184:185], v[184:185], v[200:201]\n"
"v_pk_mul_f32 v[186:187], v[186:187], v[202:203]\n"
"v_cvt_pk_bf16_f32 v164, v187, v186\n"
"v_cvt_pk_bf16_f32 v165, v185, v184\n"
"v_cvt_pk_bf16_f32 v166, v183, v182\n"
"v_cvt_pk_bf16_f32 v167, v181, v180\n"
"ds_write_b128 v210, v[164:167]\n"
"v_cvt_pk_bf16_f32 v168, v179, v178\n"
"v_cvt_pk_bf16_f32 v169, v177, v176\n"
"v_cvt_pk_bf16_f32 v170, v175, v174\n"
"v_cvt_pk_bf16_f32 v171, v173, v172\n"
"ds_write_b128 v211, v[168:171]\n"
"s_waitcnt lgkmcnt(0)\n"
"s_barrier\n"
"ds_read_b128 v[144:147], v212\n"
"ds_read_b128 v[148:151], v212 offset:1024\n"
"s_waitcnt lgkmcnt(0)\n"
"buffer_store_dwordx4 v[144:147], v214, s[24:27], s79 offen sc1\n"
"s_add_u32 s79, s79, 32768\n"
"buffer_store_dwordx4 v[148:151], v214, s[24:27], s79 offen sc1\n"
"s_add_u32 s40, s54, 16908288\n"
"s_bfe_u32 s52, s4, 0x20005\n"
"s_cmp_eq_u32 s52, 3\n"
"s_cselect_b32 s52, 128, 0\n"
"s_xor_b32 s53, s4, s52\n"
"s_lshr_b32 s41, s53, 3\n"
"s_lshl_b32 s41, s41, 12\n"
"s_lshl_b32 s52, s5, 10\n"
"s_add_u32 s41, s41, s52\n"
"s_add_u32 s41, s41, 16908288\n"
"s_lshl_b32 s52, s61, 8\n"
"s_add_u32 s52, s52, s53\n"
"s_lshl_b32 s52, s52, 7\n"
"s_and_b32 s53, s5, 2\n"
"s_lshl_b32 s53, s53, 1\n"
"s_add_u32 s52, s52, s53\n"
"s_add_u32 s52, s52, 17301504\n"
"s_mov_b32 exec_lo, 0x80000000\n"
"s_mov_b32 exec_hi, 0x80000000\n"
"buffer_store_dwordx4 v[216:219], v220, s[24:27], s83 offen sc1\n"
"s_mov_b64 exec, -1\n"
"v_mov_b32 v112, 0x3f803f80\n"
"v_mov_b32 v113, 0x3f803f80\n"
"v_mov_b32 v114, 0x3f803f80\n"
"v_mov_b32 v115, 0x3f803f80\n"
"s_waitcnt vmcnt(15)\n"
"ds_read_b64_tr_b16 a[0:1], v6 offset:32768\n"
"ds_read_b64_tr_b16 a[4:5], v7 offset:32768\n"
"ds_read_b64_tr_b16 a[2:3], v6 offset:33792\n"
"ds_read_b64_tr_b16 a[6:7], v7 offset:33792\n"
"ds_read_b64_tr_b16 a[8:9], v6 offset:34816\n"
"ds_read_b64_tr_b16 a[12:13], v7 offset:34816\n"
"ds_read_b64_tr_b16 a[10:11], v6 offset:35840\n"
"ds_read_b64_tr_b16 a[14:15], v7 offset:35840\n"
"s_waitcnt vmcnt(11)\n"
"ds_read_b64_tr_b16 a[16:17], v6 offset:36864\n"
"ds_read_b64_tr_b16 a[20:21], v7 offset:36864\n"
"ds_read_b64_tr_b16 a[18:19], v6 offset:37888\n"
"ds_read_b64_tr_b16 a[22:23], v7 offset:37888\n"
"ds_read_b64_tr_b16 a[24:25], v6 offset:38912\n"
"ds_read_b64_tr_b16 a[28:29], v7 offset:38912\n"
"ds_read_b64_tr_b16 a[26:27], v6 offset:39936\n"
"ds_read_b64_tr_b16 a[30:31], v7 offset:39936\n"
"s_waitcnt vmcnt(7)\n"
"ds_read_b64_tr_b16 a[32:33], v8 offset:0\n"
"ds_read_b64_tr_b16 a[36:37], v9 offset:0\n"
"ds_read_b64_tr_b16 a[34:35], v8 offset:1024\n"
"ds_read_b64_tr_b16 a[38:39], v9 offset:1024\n"
"ds_read_b64_tr_b16 a[40:41], v8 offset:2048\n"
"ds_read_b64_tr_b16 a[44:45], v9 offset:2048\n"
"ds_read_b64_tr_b16 a[42:43], v8 offset:3072\n"
"ds_read_b64_tr_b16 a[46:47], v9 offset:3072\n"
"s_waitcnt vmcnt(3)\n"
"ds_read_b64_tr_b16 a[48:49], v8 offset:4096\n"
"ds_read_b64_tr_b16 a[52:53], v9 offset:4096\n"
"ds_read_b64_tr_b16 a[50:51], v8 offset:5120\n"
"ds_read_b64_tr_b16 a[54:55], v9 offset:5120\n"
"ds_read_b64_tr_b16 a[56:57], v8 offset:6144\n"
"ds_read_b64_tr_b16 a[60:61], v9 offset:6144\n"
"ds_read_b64_tr_b16 a[58:59], v8 offset:7168\n"
"ds_read_b64_tr_b16 a[62:63], v9 offset:7168\n"
"s_waitcnt lgkmcnt(15)\n"
"v_mfma_f32_32x32x16_bf16 v[48:63], v[112:115], a[0:3], 0\n"
"v_mfma_f32_32x32x16_bf16 v[64:79], v[112:115], a[4:7], 0\n"
"v_mfma_f32_32x32x16_bf16 v[48:63], v[112:115], a[8:11], v[48:63]\n"
"v_mfma_f32_32x32x16_bf16 v[64:79], v[112:115], a[12:15], v[64:79]\n"
"v_mfma_f32_32x32x16_bf16 v[48:63], v[112:115], a[16:19], v[48:63]\n"
"v_mfma_f32_32x32x16_bf16 v[64:79], v[112:115], a[20:23], v[64:79]\n"
"v_mfma_f32_32x32x16_bf16 v[48:63], v[112:115], a[24:27], v[48:63]\n"
"v_mfma_f32_32x32x16_bf16 v[64:79], v[112:115], a[28:31], v[64:79]\n"
"s_waitcnt lgkmcnt(8)\n"
"v_mfma_f32_32x32x16_bf16 v[48:63], v[112:115], a[32:35], v[48:63]\n"
"v_mfma_f32_32x32x16_bf16 v[64:79], v[112:115], a[36:39], v[64:79]\n"
"v_mfma_f32_32x32x16_bf16 v[48:63], v[112:115], a[40:43], v[48:63]\n"
"v_mfma_f32_32x32x16_bf16 v[64:79], v[112:115], a[44:47], v[64:79]\n"
"s_waitcnt lgkmcnt(0)\n"
"v_mfma_f32_32x32x16_bf16 v[48:63], v[112:115], a[48:51], v[48:63]\n"
"v_mfma_f32_32x32x16_bf16 v[64:79], v[112:115], a[52:55], v[64:79]\n"
"v_mfma_f32_32x32x16_bf16 v[48:63], v[112:115], a[56:59], v[48:63]\n"
"v_mfma_f32_32x32x16_bf16 v[64:79], v[112:115], a[60:63], v[64:79]\n"
"s_lshr_b32 s50, s5, 1\n"
"v_lshrrev_b32 v116, 5, v0\n"
"v_cmp_eq_u32 vcc, s50, v116\n"
"s_lshl_b32 s50, s5, 8\n"
"v_lshl_add_u32 v117, v0, 2, s50\n"
"s_nop 6\n"
"v_cndmask_b32 v116, v64, v48, vcc\n"
"ds_write_b32 v117, v116\n"
"v_lshlrev_b32 v117, 2, v0\n"
"s_waitcnt lgkmcnt(0)\n"
"s_barrier\n"
"ds_read_b32 v118, v117\n"
"ds_read_b32 v120, v117 offset:256\n"
"ds_read_b32 v121, v117 offset:512\n"
"ds_read_b32 v122, v117 offset:768\n"
"v_mov_b32 v119, s48\n"
"s_waitcnt lgkmcnt(0)\n"
"v_add_f32 v118, v118, v120\n"
"v_add_f32 v121, v121, v122\n"
"v_add_f32 v118, v118, v121\n"
"buffer_store_dwordx2 v[118:119], v44, s[24:27], s58 offen sc1\n"
"s_waitcnt vmcnt(2)\n"
"s_mov_b64 exec, 1\n"
"buffer_store_dword v18, off, s[24:27], s40 sc1\n"
"s_mov_b64 exec, -1\n"
"s_sub_u32 s49, s4, 224\n"
"s_lshl_b32 s49, s49, 2\n"
"s_add_u32 s49, s49, s5\n"
"s_mul_i32 s50, s49, 14336\n"
"s_lshl_b32 s49, s49, 14\n"
"v_add_u32 v238, s50, v15\n"
"v_add_u32 v239, 8192, v238\n"
"buffer_load_dword v248, v238, s[28:31], 0 offen\n"
"buffer_load_dword v249, v239, s[28:31], 0 offen\n"
"s_cmp_eq_u32 s4, 255\n"
"s_cbranch_scc1 LEPX\n"
"LEPD:\n"
"s_barrier\n"
"s_lshl_b32 s49, s4, 2\n"
"s_add_u32 s49, s49, s5\n"
"s_lshl_b32 s18, s49, 7\n"
"s_add_u32 s18, s18, 17461248\n"
"s_mov_b32 s19, s18\n"
"s_cmp_eq_u32 s49, 1020\n"
"s_cselect_b32 s63, 17563648, s18\n"
"s_mov_b64 exec, 1\n"
"buffer_store_dword v18, off, s[24:27], s63 sc1\n"
"s_mov_b64 exec, -1\n"
"v_mov_b32 v21, v18\n"
"s_mov_b64 exec, 255\n"
"buffer_load_dword v21, v15, s[24:27], s41 offen sc1\n"
"s_mov_b64 exec, -1\n"
"s_mov_b32 m0, s101\n"
"s_nop 0\n"
"buffer_load_dwordx4 v1, s[32:35], s100 offen lds\n"
"buffer_load_dwordx4 v1, s[32:35], s100 offen offset:1024 lds\n"
"global_load_ushort v22, v22, s[44:45]\n"
"global_load_ushort v36, v36, s[46:47]\n"
"v_mov_b32 v224, 0\n"
"v_mov_b32 v225, 0\n"
"v_mov_b32 v226, 0\n"
"v_mov_b32 v227, 0\n"
"ds_write_b128 v40, v[224:227]\n"
"s_waitcnt vmcnt(4)\n"
"v_cmp_ne_u32 vcc, v18, v21\n"
"s_cbranch_vccnz LS3\n"
"LB3:\n"
"s_barrier\n"
"buffer_load_dwordx4 v[160:163], v1, s[24:27], s3 offen offset:0 sc1\n"
"buffer_load_dwordx4 v[168:171], v1, s[24:27], s3 offen offset:1024 sc1\n"
"s_mov_b32 m0, s0\n"
"s_nop 0\n"
"buffer_load_dwordx4 v2, s[28:31], s64 offen offset:0 lds\n"
"buffer_load_dwordx4 v3, s[28:31], s64 offen offset:1024 lds\n"
"buffer_load_dwordx4 v4, s[28:31], s64 offen offset:2048 lds\n"
"buffer_load_dwordx4 v5, s[28:31], s64 offen offset:3072 lds\n"
"s_add_u32 m0, s78, 32768\n"
"s_nop 0\n"
"buffer_load_dwordx4 v2, s[28:31], s65 offen offset:0 lds\n"
"buffer_load_dwordx4 v3, s[28:31], s65 offen offset:1024 lds\n"
"buffer_load_dwordx4 v4, s[28:31], s65 offen offset:2048 lds\n"
"buffer_load_dwordx4 v5, s[28:31], s65 offen offset:3072 lds\n"
"s_waitcnt vmcnt(8)\n"
"s_add_u32 m0, s78, 36864\n"
"s_nop 0\n"
"buffer_load_dwordx4 v2, s[28:31], s66 offen offset:0 lds\n"
"ds_write_b128 v39, v[160:163] offset:0\n"
"ds_write_b128 v39, v[168:171] offset:1024\n"
"s_waitcnt lgkmcnt(0)\n"
"s_barrier\n"
"buffer_load_dwordx4 v3, s[28:31], s66 offen offset:1024 lds\n"
"ds_read_b64 v[164:165], v39 offset:16\n"
"ds_read_b64 v[172:173], v39 offset:1040\n"
"s_waitcnt lgkmcnt(1)\n"
"v_alignbit_b32 v224, v161, v160, 16\n"
"buffer_load_dwordx4 v4, s[28:31], s66 offen offset:2048 lds\n"
"v_alignbit_b32 v225, v162, v161, 16\n"
"v_alignbit_b32 v226, v163, v162, 16\n"
"v_alignbit_b32 v227, v164, v163, 16\n"
"ds_write_b128 v39, v[224:227] offset:4512\n"
"buffer_load_dwordx4 v5, s[28:31], s66 offen offset:3072 lds\n"
"v_mov_b32 v228, v161\n"
"v_mov_b32 v229, v162\n"
"v_mov_b32 v230, v163\n"
"v_mov_b32 v231, v164\n"
"s_add_u32 m0, s78, 65536\n"
"s_nop 0\n"
"buffer_load_dwordx4 v2, s[28:31], s67 offen offset:0 lds\n"
"ds_write_b128 v39, v[228:231] offset:9024\n"
"v_alignbit_b32 v232, v162, v161, 16\n"
"v_alignbit_b32 v233, v163, v162, 16\n"
"v_alignbit_b32 v234, v164, v163, 16\n"
"buffer_load_dwordx4 v3, s[28:31], s67 offen offset:1024 lds\n"
"v_alignbit_b32 v235, v165, v164, 16\n"
"ds_write_b128 v39, v[232:235] offset:13536\n"
"s_waitcnt lgkmcnt(3)\n"
"v_alignbit_b32 v224, v169, v168, 16\n"
"buffer_load_dwordx4 v4, s[28:31], s67 offen offset:2048 lds\n"
"v_alignbit_b32 v225, v170, v169, 16\n"
"v_alignbit_b32 v226, v171, v170, 16\n"
"v_alignbit_b32 v227, v172, v171, 16\n"
"ds_write_b128 v39, v[224:227] offset:5536\n"
"buffer_load_dwordx4 v5, s[28:31], s67 offen offset:3072 lds\n"
"v_mov_b32 v228, v169\n"
"v_mov_b32 v229, v170\n"
"v_mov_b32 v230, v171\n"
"v_mov_b32 v231, v172\n"
"s_add_u32 m0, s78, 69632\n"
"s_nop 0\n"
"buffer_load_dwordx4 v2, s[28:31], s68 offen offset:0 lds\n"
"ds_write_b128 v39, v[228:231] offset:10048\n"
"v_alignbit_b32 v232, v170, v169, 16\n"
"v_alignbit_b32 v233, v171, v170, 16\n"
"v_alignbit_b32 v234, v172, v171, 16\n"
"buffer_load_dwordx4 v3, s[28:31], s68 offen offset:1024 lds\n"
"v_alignbit_b32 v235, v173, v172, 16\n"
"ds_write_b128 v39, v[232:235] offset:14560\n"
"buffer_load_dwordx4 v4, s[28:31], s68 offen offset:2048 lds\n"
"buffer_load_dwordx4 v5, s[28:31], s68 offen offset:3072 lds\n"
"s_add_u32 m0, s78, 98304\n"
"s_nop 0\n"
"buffer_load_dwordx4 v2, s[28:31], s69 offen offset:0 lds\n"
"buffer_load_dwordx4 v3, s[28:31], s69 offen offset:1024 lds\n"
"buffer_load_dwordx4 v4, s[28:31], s69 offen offset:2048 lds\n"
"buffer_load_dwordx4 v5, s[28:31], s69 offen offset:3072 lds\n"
"s_add_u32 m0, s78, 102400\n"
"s_nop 0\n"
"buffer_load_dwordx4 v2, s[28:31], s70 offen offset:0 lds\n"
"buffer_load_dwordx4 v3, s[28:31], s70 offen offset:1024 lds\n"
"buffer_load_dwordx4 v4, s[28:31], s70 offen offset:2048 lds\n"
"buffer_load_dwordx4 v5, s[28:31], s70 offen offset:3072 lds\n"
"s_waitcnt vmcnt(24)\n"
"ds_read_b64_tr_b16 a[0:1], v10 offset:0\n"
"ds_read_b64_tr_b16 a[4:5], v11 offset:0\n"
"ds_read_b64_tr_b16 a[2:3], v10 offset:1024\n"
"ds_read_b64_tr_b16 a[6:7], v11 offset:1024\n"
"ds_read_b64_tr_b16 a[8:9], v10 offset:2048\n"
"ds_read_b64_tr_b16 a[12:13], v11 offset:2048\n"
"ds_read_b64_tr_b16 a[10:11], v10 offset:3072\n"
"ds_read_b64_tr_b16 a[14:15], v11 offset:3072\n"
"s_waitcnt lgkmcnt(0)\n"
"s_mov_b32 m0, s0\n"
"s_add_u32 s64, s64, 32768\n"
"buffer_load_dwordx4 v2, s[28:31], s64 offen offset:0 lds\n"
"buffer_load_dwordx4 v3, s[28:31], s64 offen offset:1024 lds\n"
"buffer_load_dwordx4 v4, s[28:31], s64 offen offset:2048 lds\n"
"buffer_load_dwordx4 v5, s[28:31], s64 offen offset:3072 lds\n"
"s_waitcnt vmcnt(24)\n"
"ds_read_b64_tr_b16 a[16:17], v6 offset:32768\n"
"ds_read_b64_tr_b16 a[20:21], v7 offset:32768\n"
"ds_read_b64_tr_b16 a[18:19], v6 offset:33792\n"
"ds_read_b64_tr_b16 a[22:23], v7 offset:33792\n"
"ds_read_b64_tr_b16 a[24:25], v6 offset:34816\n"
"ds_read_b64_tr_b16 a[28:29], v7 offset:34816\n"
"ds_read_b64_tr_b16 a[26:27], v6 offset:35840\n"
"ds_read_b64_tr_b16 a[30:31], v7 offset:35840\n"
"s_waitcnt lgkmcnt(0)\n"
"s_add_u32 m0, s78, 32768\n"
"s_add_u32 s65, s65, 32768\n"
"buffer_load_dwordx4 v2, s[28:31], s65 offen offset:0 lds\n"
"buffer_load_dwordx4 v3, s[28:31], s65 offen offset:1024 lds\n"
"buffer_load_dwordx4 v4, s[28:31], s65 offen offset:2048 lds\n"
"buffer_load_dwordx4 v5, s[28:31], s65 offen offset:3072 lds\n"
"s_mov_b32 m0, 31744\n"
"s_mov_b64 exec, 1\n"
"buffer_load_dword v1, s[28:31], 0 offen lds\n"
"buffer_load_dword v1, s[28:31], 0 offen lds\n"
"buffer_load_dword v1, s[28:31], 0 offen lds\n"
"buffer_load_dword v1, s[28:31], 0 offen lds\n"
"buffer_load_dword v1, s[28:31], 0 offen lds\n"
"buffer_load_dword v1, s[28:31], 0 offen lds\n"
"buffer_load_dword v1, s[28:31], 0 offen lds\n"
"buffer_load_dword v1, s[28:31], 0 offen lds\n"
"buffer_load_dword v1, s[28:31], 0 offen lds\n"
"buffer_load_dword v1, s[28:31], 0 offen lds\n"
"s_mov_b64 exec, -1\n"
"s_waitcnt lgkmcnt(0)\n"
"s_waitcnt lgkmcnt(0)\n"
"s_branch LJOIN\n"
"LS0:\n"
"s_mov_b32 s62, 0\n"
"LL0:\n"
"s_sleep 2\n"
"v_mov_b32 v21, v18\n"
"s_mov_b64 exec, 255\n"
"buffer_load_dword v21, v15, s[24:27], s41 offen sc1\n"
"s_mov_b64 exec, -1\n"
"s_waitcnt vmcnt(0)\n"
"v_cmp_ne_u32 vcc, v18, v21\n"
"s_cbranch_vccz LB0\n"
"s_add_u32 s62, s62, 1\n"
"s_cmp_lt_u32 s62, 0x8000\n"
"s_cbranch_scc1 LL0\n"
"s_branch LB0\n"
"LS1:\n"
"s_mov_b32 s62, 0\n"
"LL1:\n"
"s_sleep 2\n"
"buffer_load_dwordx4 v[200:203], v37, s[24:27], s22 offen sc1\n"
"buffer_load_dwordx4 v[204:207], v37, s[24:27], s22 offen offset:512 sc1\n"
"s_waitcnt vmcnt(0)\n"
"v_max3_u32 v212, v201, v203, v205\n"
"v_min3_u32 v213, v201, v203, v205\n"
"v_max3_u32 v212, v212, v207, v18\n"
"v_min3_u32 v213, v213, v207, v18\n"
"v_cmp_ne_u32 vcc, v212, v213\n"
"s_cbranch_vccz LB1\n"
"s_add_u32 s62, s62, 1\n"
"s_cmp_lt_u32 s62, 0x8000\n"
"s_cbranch_scc1 LL1\n"
"s_branch LB1\n"
"LS2:\n"
"s_mov_b32 s62, 0\n"
"LL2:\n"
"s_sleep 2\n"
"buffer_load_dwordx2 v[208:209], v42, s[24:27], s23 offen sc1\n"
"buffer_load_dwordx2 v[210:211], v42, s[24:27], s23 offen offset:256 sc1\n"
"s_waitcnt vmcnt(0)\n"
"v_max3_u32 v222, v209, v211, v18\n"
"v_min3_u32 v223, v209, v211, v18\n"
"v_cmp_ne_u32 vcc, v222, v223\n"
"s_cbranch_vccz LB2\n"
"s_add_u32 s62, s62, 1\n"
"s_cmp_lt_u32 s62, 0x8000\n"
"s_cbranch_scc1 LL2\n"
"s_branch LB2\n"
"LEPX:\n"
"s_mov_b32 s62, 0\n"
"v_lshlrev_b32 v112, 5, v0\n"
"s_add_u32 s63, s78, 16777220\n"
"s_add_u32 s51, s63, 4096\n"
"LEPL:\n"
"buffer_load_dword v116, v112, s[24:27], s63 offen sc1\n"
"buffer_load_dword v117, v112, s[24:27], s63 offen offset:2048 sc1\n"
"buffer_load_dword v120, v112, s[24:27], s51 offen sc1\n"
"buffer_load_dword v121, v112, s[24:27], s51 offen offset:2048 sc1\n"
"s_waitcnt vmcnt(0)\n"
"v_max3_u32 v114, v18, v116, v117\n"
"v_min3_u32 v115, v18, v116, v117\n"
"v_max3_u32 v114, v114, v120, v121\n"
"v_min3_u32 v115, v115, v120, v121\n"
"v_cmp_ne_u32 vcc, v114, v115\n"
"s_add_u32 s62, s62, 1\n"
"s_cmp_gt_u32 s62, 0x8000\n"
"s_cbranch_scc1 LEPD\n"
"s_cbranch_vccnz LEPL\n"
"s_branch LEPD\n"
"LS3:\n"
"s_mov_b32 s62, 0\n"
"LL3:\n"
"s_sleep 2\n"
"v_mov_b32 v21, v18\n"
"s_mov_b64 exec, 255\n"
"buffer_load_dword v21, v15, s[24:27], s41 offen sc1\n"
"s_mov_b64 exec, -1\n"
"s_waitcnt vmcnt(0)\n"
"v_cmp_ne_u32 vcc, v18, v21\n"
"s_cbranch_vccz LB3\n"
"s_add_u32 s62, s62, 1\n"
"s_cmp_lt_u32 s62, 0x8000\n"
"s_cbranch_scc1 LL3\n"
"s_branch LB3\n"
"LS4:\n"
"s_mov_b32 s62, 0\n"
"LL4:\n"
"s_sleep 2\n"
"v_mov_b32 v21, v18\n"
"s_mov_b64 exec, 127\n"
"buffer_load_dword v21, v15, s[24:27], s1 offen sc1\n"
"s_mov_b64 exec, -1\n"
"s_waitcnt vmcnt(0)\n"
"v_cmp_ne_u32 vcc, v18, v21\n"
"s_cbranch_vccz LB4\n"
"s_add_u32 s62, s62, 1\n"
"s_cmp_lt_u32 s62, 0x8000\n"
"s_cbranch_scc1 LL4\n"
"s_branch LB4\n"
"LS5:\n"
"s_mov_b32 s62, 0\n"
"LL5:\n"
"s_sleep 2\n"
"v_mov_b32 v21, v18\n"
"s_mov_b64 exec, 127\n"
"buffer_load_dword v21, v15, s[24:27], s2 offen sc1\n"
"s_mov_b64 exec, -1\n"
"s_waitcnt vmcnt(0)\n"
"v_cmp_ne_u32 vcc, v18, v21\n"
"s_cbranch_vccz LB5\n"
"s_add_u32 s62, s62, 1\n"
"s_cmp_lt_u32 s62, 0x8000\n"
"s_cbranch_scc1 LL5\n"
"s_branch LB5\n"
"LCX1:\n"
"s_nop 5\n"
"ds_write_b128 v19, v[224:227] offset:0\n"
"v_pk_fma_f32 v[124:125], v[56:57], v[22:23], v[240:241] op_sel:[0,0,0] op_sel_hi:[1,0,1]\n"
"v_pk_fma_f32 v[126:127], v[58:59], v[22:23], v[242:243] op_sel:[0,0,0] op_sel_hi:[1,0,1]\n"
"ds_write_b128 v19, v[228:231] offset:1024\n"
"v_pk_fma_f32 v[128:129], v[60:61], v[22:23], v[244:245] op_sel:[0,0,0] op_sel_hi:[1,0,1]\n"
"v_pk_fma_f32 v[130:131], v[62:63], v[22:23], v[246:247] op_sel:[0,0,0] op_sel_hi:[1,0,1]\n"
"ds_write_b128 v19, v[232:235] offset:2048\n"
"v_pk_fma_f32 v[132:133], v[64:65], v[22:23], v[248:249] op_sel:[0,0,0] op_sel_hi:[1,0,1]\n"
"v_pk_fma_f32 v[134:135], v[66:67], v[22:23], v[250:251] op_sel:[0,0,0] op_sel_hi:[1,0,1]\n"
"ds_write_b128 v19, v[236:239] offset:3072\n"
"v_pk_fma_f32 v[136:137], v[68:69], v[22:23], v[252:253] op_sel:[0,0,0] op_sel_hi:[1,0,1]\n"
"v_pk_fma_f32 v[138:139], v[70:71], v[22:23], v[254:255] op_sel:[0,0,0] op_sel_hi:[1,0,1]\n"
"s_branch LCX2\n"
"LS6:\n"
"s_mov_b32 s62, 0\n"
"LL6:\n"
"s_sleep 2\n"
"s_mov_b32 s49, 16777216\n"
"buffer_load_dwordx4 v[120:123], v46, s[24:27], s49 offen sc1\n"
"s_waitcnt vmcnt(0)\n"
"v_xor_b32 v164, v121, v18\n"
"v_xor_b32 v165, v123, v18\n"
"v_or_b32 v164, v164, v165\n"
"v_cmp_ne_u32 vcc, 0, v164\n"
"s_cbranch_vccz LB6\n"
"s_add_u32 s62, s62, 1\n"
"s_cmp_lt_u32 s62, 0x8000\n"
"s_cbranch_scc1 LL6\n"
"s_branch LB6\n"
"LS7:\n"
"s_mov_b32 s62, 0\n"
"LL7:\n"
"s_sleep 2\n"
"v_mov_b32 v174, v18\n"
"v_mov_b32 v175, v18\n"
"s_mov_b32 s50, 17301504\n"
"s_mov_b64 exec, 65535\n"
"buffer_load_dwordx2 v[174:175], v45, s[24:27], s50 offen sc1\n"
"s_mov_b64 exec, -1\n"
"s_waitcnt vmcnt(0)\n"
"v_xor_b32 v174, v174, v18\n"
"v_xor_b32 v175, v175, v18\n"
"v_or_b32 v174, v174, v175\n"
"v_cmp_ne_u32 vcc, 0, v174\n"
"s_cbranch_vccz LB7\n"
"s_add_u32 s62, s62, 1\n"
"s_cmp_lt_u32 s62, 0x8000\n"
"s_cbranch_scc1 LL7\n"
"s_branch LB7\n"
"LFIN:\n"
"s_endpgm\n"
".p2align 12\n"
  :: "s"(__builtin_amdgcn_kernarg_segment_ptr()), "s"(__builtin_amdgcn_workgroup_id_x()), "v"(__builtin_amdgcn_workitem_id_x())
  : "v1","v2","v3","v4","v5","v6","v7","v8","v9","v10","v11","v12","v13","v14","v15","v16","v17","v18","v19","v20","v21","v22","v23","v24","v25","v26","v27","v28","v29","v30","v31","v32","v33","v34","v35","v36","v37","v38","v39","v40","v41","v42","v43","v44","v45","v46","v47","v48","v49","v50","v51","v52","v53","v54","v55","v56","v57","v58","v59","v60","v61","v62","v63","v64","v65","v66","v67","v68","v69","v70","v71","v72","v73","v74","v75","v76","v77","v78","v79","v80","v81","v82","v83","v84","v85","v86","v87","v88","v89","v90","v91","v92","v93","v94","v95","v96","v97","v98","v99","v100","v101","v102","v103","v104","v105","v106","v107","v108","v109","v110","v111","v112","v113","v114","v115","v116","v117","v118","v119","v120","v121","v122","v123","v124","v125","v126","v127","v128","v129","v130","v131","v132","v133","v134","v135","v136","v137","v138","v139","v140","v141","v142","v143","v144","v145","v146","v147","v148","v149","v150","v151","v152","v153","v154","v155","v156","v157","v158","v159","v160","v161","v162","v163","v164","v165","v166","v167","v168","v169","v170","v171","v172","v173","v174","v175","v176","v177","v178","v179","v180","v181","v182","v183","v184","v185","v186","v187","v188","v189","v190","v191","v192","v193","v194","v195","v196","v197","v198","v199","v200","v201","v202","v203","v204","v205","v206","v207","v208","v209","v210","v211","v212","v213","v214","v215","v216","v217","v218","v219","v220","v221","v222","v223","v224","v225","v226","v227","v228","v229","v230","v231","v232","v233","v234","v235","v236","v237","v238","v239","v240","v241","v242","v243","v244","v245","v246","v247","v248","v249","v250","v251","v252","v253","v254","v255","a0","a1","a2","a3","a4","a5","a6","a7","a8","a9","a10","a11","a12","a13","a14","a15","a16","a17","a18","a19","a20","a21","a22","a23","a24","a25","a26","a27","a28","a29","a30","a31","a32","a33","a34","a35","a36","a37","a38","a39","a40","a41","a42","a43","a44","a45","a46","a47","a48","a49","a50","a51","a52","a53","a54","a55","a56","a57","a58","a59","a60","a61","a62","a63","a64","a65","a66","a67","a68","a69","a70","a71","a72","a73","a74","a75","a76","a77","a78","a79","a80","a81","a82","a83","a84","a85","a86","a87","a88","a89","a90","a91","a92","a93","a94","a95","a96","a97","a98","a99","a100","a101","a102","a103","a104","a105","a106","a107","a108","a109","a110","a111","a112","a113","a114","a115","a116","a117","a118","a119","a120","a121","a122","a123","a124","a125","a126","a127","a128","a129","a130","a131","a132","a133","a134","a135","a136","a137","a138","a139","a140","a141","a142","a143","a144","a145","a146","a147","a148","a149","a150","a151","a152","a153","a154","a155","a156","a157","a158","a159","a160","a161","a162","a163","a164","a165","a166","a167","a168","a169","a170","a171","a172","a173","a174","a175","a176","a177","a178","a179","a180","a181","a182","a183","a184","a185","a186","a187","a188","a189","a190","a191","a192","a193","a194","a195","a196","a197","a198","a199","a200","a201","a202","a203","a204","a205","a206","a207","a208","a209","a210","a211","a212","a213","a214","a215","a216","a217","a218","a219","a220","a221","a222","a223","a224","a225","a226","a227","a228","a229","a230","a231","a232","a233","a234","a235","a236","a237","a238","a239","a240","a241","a242","a243","a244","a245","a246","a247","a248","a249","a250","a251","a252","a253","a254","a255","s4","s5","s6","s7","s8","s9","s10","s11","s12","s13","s14","s15","s16","s17","s18","s19","s20","s21","s22","s23","s24","s25","s26","s27","s28","s29","s30","s31","s32","s33","s34","s35","s36","s37","s38","s39","s40","s41","s42","s43","s44","s45","s46","s47","s48","s49","s50","s51","s52","s53","s54","s55","s56","s57","s58","s59","s60","s61","s62","s63","s64","s65","s66","s67","s68","s69","s70","s71","s72","s73","s74","s75","s76","s77","s78","s79","s80","s81","s82","s83","s84","s85","s86","s87","s88","s89","s90","s91","s92","s93","s94","s95","s96","s97","s98","s99","s100","s101","vcc","scc","memory");
}

extern "C" void kernel_launch(void* const* d_in, const int* in_sizes, int n_in, void* d_out, int out_size, void* d_ws, size_t ws_size,
                              hipStream_t stream) {
  const u32 F = 0x20000u, A = 2048u * 512u * 2u, W = 512u * 1792u * 2u;
  const u32 wsb = ws_size > (size_t)(256u << 20) ? (256u << 20) : (u32)ws_size;
  (void)hipFuncSetAttribute((const void*)fused_kernel, hipFuncAttributeMaxDynamicSharedMemorySize, 163840);
  fused_kernel<<<256, 256, 163840, stream>>>(d_in[0], A, F, d_in[7], W, F, d_in[1], A, F, d_in[2], A, F, d_ws, wsb, F, d_in[9], W, F,
                                             d_in[4], A, F, d_out, A, F, d_in[3], d_in[8], d_in[5], d_in[10]);
}
```
